```python
import torch

def _softmax(x, axis=-1):
    return torch.softmax(x, dim=axis)
def _gelu(x):
    return torch.nn.functional.gelu(x, approximate="tanh")
def _silu(x):
    return torch.nn.functional.silu(x)
def _sigmoid(x):
    return torch.sigmoid(x)
def _layernorm(x, axis=-1, eps=1e-5):
    m = x.mean(dim=axis, keepdim=True)
    v = x.var(dim=axis, keepdim=True, unbiased=False)
    return (x - m) / torch.sqrt(v + eps)
def _scatter_add(tgt, idx, src, axis=0):
    return tgt.clone().index_add_(axis, idx, src)
def _segment_sum(src, seg, n):
    out = torch.zeros((n,) + src.shape[1:], dtype=src.dtype)
    return out.index_add_(0, seg, src)
def _pad(x, pw):
    flat = tuple(v for pair in reversed(pw) for v in pair)
    return torch.nn.functional.pad(x, flat)

kernel_name = 'standalone__paged_block_table_kv_pagedattention__decode'

def setup_inputs():

    _w = (lambda a: (lambda t: t.to(torch.bfloat16) if t.is_floating_point() else t)(torch.as_tensor(a)))
    B, Hq, Hkv, Sq, Skv, D = 1, 32, 8, 1, 32768, 128
    bs = 16
    nbps = max(1, Skv // bs)
    num_phys = B * nbps + 4
    bt = torch.randperm(num_phys)[: B * nbps].reshape(B, nbps).to(torch.int64)
    cl = (nbps * bs - (torch.arange(1, B + 1, dtype=torch.int64) * 2654435761) % max(1, nbps * bs // 2)).to(torch.int64)
    return {
        "q": _w((lambda *s: torch.randn(*s, dtype=torch.bfloat16) * 0.8)(B, Hq, Sq, D)),
        "k_cache": _w((lambda *s: torch.randn(*s, dtype=torch.bfloat16) * 0.8)(num_phys, bs, Hkv, D)),
        "v_cache": _w((lambda *s: torch.randn(*s, dtype=torch.bfloat16) * 0.05)(num_phys, bs, Hkv, D)),
        "block_table": _w(bt),
        "context_lens": _w(cl),
    }

def reference(q, k_cache, v_cache, block_table, context_lens):
    B, Hq, Sq, D = q.shape
    bs = k_cache.shape[1]
    Hkv = k_cache.shape[2]
    nbps = block_table.shape[1]
    Skv = nbps * bs
    bt = block_table.reshape(-1)
    k = (lambda x, *d: x.permute(*d))(k_cache[bt].reshape(B, Skv, Hkv, D), 0, 2, 1, 3)
    v = (lambda x, *d: x.permute(*d))(v_cache[bt].reshape(B, Skv, Hkv, D), 0, 2, 1, 3)
    g = Hq // Hkv
    qg = q.reshape(B, Hkv, g, Sq, D)
    scores = torch.einsum("bhgqd,bhkd->bhgqk", qg, k) * (D ** -0.5)
    pos = torch.arange(Skv)
    msk = pos[None, :] < context_lens[:, None]
    scores = torch.where(msk[:, None, None, None, :], scores, -1e9)
    p = _softmax(scores, axis=-1)
    out = torch.einsum("bhgqk,bhkd->bhgqd", p, v)
    return out.reshape(B, Hq, Sq, D)
```

```cpp
#include <hip/hip_runtime.h>
#include <hip/hip_bf16.h>
#include <stdint.h>

typedef unsigned u32x4 __attribute__((ext_vector_type(4)));
typedef unsigned u32x2 __attribute__((ext_vector_type(2)));
typedef float f32x4 __attribute__((ext_vector_type(4)));
typedef short s16x4 __attribute__((ext_vector_type(4)));
typedef __bf16 bf16x8 __attribute__((ext_vector_type(8)));

#define KV_AUX 2
#define WIN 5

constexpr int HQ = 32, HKV = 8, G = 4, D = 128, PAGE = 16, NBPS = 2048;
constexpr int NWAVE = 8, NSPLIT = 32, WPH = NWAVE * NSPLIT;
constexpr int PPW = NBPS / WPH;
constexpr int ROWB = HKV * D * 2;
constexpr int PAGEB = PAGE * ROWB;
constexpr float C2 = 0.088388347648318440f * 1.4426950408889634f;
static_assert(PPW == 8, "prologue loads 8 block-table entries");

template <int CTRL> __device__ __forceinline__ float dpp(float x) {
  return __builtin_bit_cast(float, __builtin_amdgcn_mov_dpp(__builtin_bit_cast(int, x), CTRL, 0xf, 0xf, true));
}
__device__ __forceinline__ float xrow16_max(float x) {
  auto s = __builtin_amdgcn_permlane16_swap(__float_as_uint(x), __float_as_uint(x), false, false);
  x = fmaxf(__uint_as_float(s[0]), __uint_as_float(s[1]));
  auto t = __builtin_amdgcn_permlane32_swap(__float_as_uint(x), __float_as_uint(x), false, false);
  return fmaxf(__uint_as_float(t[0]), __uint_as_float(t[1]));
}
__device__ __forceinline__ float xrow16_sum(float x) {
  auto s = __builtin_amdgcn_permlane16_swap(__float_as_uint(x), __float_as_uint(x), false, false);
  x = __uint_as_float(s[0]) + __uint_as_float(s[1]);
  auto t = __builtin_amdgcn_permlane32_swap(__float_as_uint(x), __float_as_uint(x), false, false);
  return __uint_as_float(t[0]) + __uint_as_float(t[1]);
}
__device__ __forceinline__ unsigned cvtpk(float lo, float hi) {
  typedef __bf16 bf16x2_t __attribute__((ext_vector_type(2)));
  typedef float f32x2_t __attribute__((ext_vector_type(2)));
  f32x2_t v = {lo, hi};
  return __builtin_bit_cast(unsigned, __builtin_convertvector(v, bf16x2_t));
}

__global__ void __launch_bounds__(NWAVE * 64, 2)
attn_split(const unsigned char* __restrict__ q, const unsigned char* __restrict__ kc, const unsigned char* __restrict__ vc,
           const int* __restrict__ bt, const int* __restrict__ lens, float* __restrict__ po) {
  __shared__ __attribute__((aligned(16))) float lds[NWAVE * 512 + 2 * NWAVE * 4 + NWAVE * 4];
  float* lds_acc = lds;
  float* lds_m = lds + NWAVE * 512;
  float* lds_l = lds_m + 2 * NWAVE * 4;
  float* pml = po + HQ * NSPLIT * D;
  const int h = blockIdx.x & 7, s = blockIdx.x >> 3;
  const int tid = threadIdx.x, lane = tid & 63;
  const int wave = __builtin_amdgcn_readfirstlane(tid >> 6);
  const int widx = wave * NSPLIT + s;
  const int kg = lane >> 4, t4 = (lane >> 2) & 3, c4 = lane & 3, c16 = lane & 15;
  const u32x4 qf = *reinterpret_cast<const u32x4*>(q + (h * G + t4) * (D * 2) + kg * 64 + c4 * 16);

  unsigned ph[PPW + 2];
  int len;
  {
    const int* btw = bt + widx;
    asm volatile(
        "s_load_dword %0, %10, 0x0\n\t"
        "s_load_dword %1, %9, 0x0\n\t"
        "s_load_dword %2, %9, 0x400\n\t"
        "s_load_dword %3, %9, 0x800\n\t"
        "s_load_dword %4, %9, 0xc00\n\t"
        "s_load_dword %5, %9, 0x1000\n\t"
        "s_load_dword %6, %9, 0x1400\n\t"
        "s_load_dword %7, %9, 0x1800\n\t"
        "s_load_dword %8, %9, 0x1c00\n\t"
        "s_waitcnt lgkmcnt(0)"
        : "=&s"(len), "=&s"(ph[0]), "=&s"(ph[1]), "=&s"(ph[2]), "=&s"(ph[3]), "=&s"(ph[4]), "=&s"(ph[5]), "=&s"(ph[6]), "=&s"(ph[7])
        : "s"(btw), "s"(lens));
    ph[8] = 0; ph[9] = 0;
  }
  len = min(len, NBPS * PAGE);
  const int nwin = (len > WPH * WIN * PAGE) ? 2 : 1;
  static_assert(2 * WIN >= PPW, "two windows cover all pages");

  const unsigned koffA = h * (D * 2) + t4 * 4 * ROWB + kg * 64 + c4 * 16, koffB = koffA + 2 * ROWB;
  const unsigned voffA = h * (D * 2) + kg * 4 * ROWB + c16 * 16, voffB = voffA + 2 * ROWB;

  u32x4 K[WIN][4], V[WIN][4];
  f32x4 acc[8];
#pragma unroll
  for (int j = 0; j < 8; ++j) acc[j] = f32x4{0.f, 0.f, 0.f, 0.f};
  float m = -INFINITY, l = 0.f;

  int rem0 = len - widx * PAGE;
  int w = 0;
  do {
#pragma unroll
    for (int i = 0; i < WIN; ++i) {
      const unsigned off = ph[i] * PAGEB;
      const unsigned nrec = off + (unsigned)max(rem0 - i * WPH * PAGE, 0) * ROWB;
      const __amdgpu_buffer_rsrc_t r = __builtin_amdgcn_make_buffer_rsrc((void*)kc, 0, nrec, 0x00020000);
      K[i][0] = __builtin_amdgcn_raw_buffer_load_b128(r, koffA, off, KV_AUX);
      K[i][1] = __builtin_amdgcn_raw_buffer_load_b128(r, koffA + ROWB, off, KV_AUX);
      K[i][2] = __builtin_amdgcn_raw_buffer_load_b128(r, koffB, off, KV_AUX);
      K[i][3] = __builtin_amdgcn_raw_buffer_load_b128(r, koffB + ROWB, off, KV_AUX);
    }
#pragma unroll
    for (int i = 0; i < WIN; ++i) {
      const unsigned off = ph[i] * PAGEB;
      const unsigned nrec = off + (unsigned)max(rem0 - i * WPH * PAGE, 0) * ROWB;
      const __amdgpu_buffer_rsrc_t r = __builtin_amdgcn_make_buffer_rsrc((void*)vc, 0, nrec, 0x00020000);
      V[i][0] = __builtin_amdgcn_raw_buffer_load_b128(r, voffA, off, KV_AUX);
      V[i][1] = __builtin_amdgcn_raw_buffer_load_b128(r, voffA + ROWB, off, KV_AUX);
      V[i][2] = __builtin_amdgcn_raw_buffer_load_b128(r, voffB, off, KV_AUX);
      V[i][3] = __builtin_amdgcn_raw_buffer_load_b128(r, voffB + ROWB, off, KV_AUX);
    }
    __builtin_amdgcn_sched_barrier(0);
    float S[WIN][4];
    float mx = -INFINITY;
#pragma unroll
    for (int i = 0; i < WIN; ++i) {
      const int remi = rem0 - i * WPH * PAGE - 4 * kg;
#pragma unroll
      for (int t = 0; t < 4; ++t) {
        f32x4 d = __builtin_amdgcn_mfma_f32_16x16x32_bf16(__builtin_bit_cast(bf16x8, K[i][t]), __builtin_bit_cast(bf16x8, qf), f32x4{0.f, 0.f, 0.f, 0.f}, 0, 0, 0);
        float x = dpp<0x00>(d[0]);
        x += dpp<0x55>(d[1]);
        x += dpp<0xAA>(d[2]);
        x += dpp<0xFF>(d[3]);
        x = (t < remi) ? x * C2 : -INFINITY;
        S[i][t] = x;
        mx = fmaxf(mx, x);
      }
    }
    mx = xrow16_max(mx);
    float* lm = lds_m + (w & 1) * (NWAVE * 4);
    if (lane < 16 && c4 == 0) lm[t4 * NWAVE + wave] = mx;
    __syncthreads();
    {
      const f32x4 a = *reinterpret_cast<const f32x4*>(lm + t4 * NWAVE);
      const f32x4 b = *reinterpret_cast<const f32x4*>(lm + t4 * NWAVE + 4);
      mx = fmaxf(fmaxf(fmaxf(a[0], a[1]), fmaxf(a[2], a[3])), fmaxf(fmaxf(b[0], b[1]), fmaxf(b[2], b[3])));
    }
    const float mn = fmaxf(m, mx);
    const float msub = (mn == -INFINITY) ? 0.f : mn;
    {
      const float alpha = __builtin_amdgcn_exp2f(m - msub);
      l *= alpha;
#pragma unroll
      for (int j = 0; j < 8; ++j) acc[j] *= alpha;
    }
    m = mn;
    u32x2 P[WIN];
#pragma unroll
    for (int i = 0; i < WIN; ++i) {
      float p[4];
#pragma unroll
      for (int t = 0; t < 4; ++t) { p[t] = __builtin_amdgcn_exp2f(S[i][t] - msub); l += p[t]; }
      P[i] = u32x2{cvtpk(p[0], p[1]), cvtpk(p[2], p[3])};
    }
    __builtin_amdgcn_sched_barrier(0);
#pragma unroll
    for (int i = 0; i < WIN; ++i) {
#pragma unroll
      for (int wd = 0; wd < 4; ++wd) {
        const u32x2 lo = {__builtin_amdgcn_perm(V[i][1][wd], V[i][0][wd], 0x05040100u), __builtin_amdgcn_perm(V[i][3][wd], V[i][2][wd], 0x05040100u)};
        const u32x2 hi = {__builtin_amdgcn_perm(V[i][1][wd], V[i][0][wd], 0x07060302u), __builtin_amdgcn_perm(V[i][3][wd], V[i][2][wd], 0x07060302u)};
        acc[2 * wd] = __builtin_amdgcn_mfma_f32_16x16x16bf16_1k(__builtin_bit_cast(s16x4, lo), __builtin_bit_cast(s16x4, P[i]), acc[2 * wd], 0, 0, 0);
        acc[2 * wd + 1] = __builtin_amdgcn_mfma_f32_16x16x16bf16_1k(__builtin_bit_cast(s16x4, hi), __builtin_bit_cast(s16x4, P[i]), acc[2 * wd + 1], 0, 0, 0);
      }
    }
#pragma unroll
    for (int i = 0; i < WIN; ++i) ph[i] = ph[i + WIN];
    rem0 -= WIN * WPH * PAGE;
  } while (++w < nwin);
  l = xrow16_sum(l);
  if (c4 == 0) {
    float* dst = lds_acc + wave * 512 + (t4 * 4 + kg) * 32;
#pragma unroll
    for (int j = 0; j < 8; ++j) *reinterpret_cast<f32x4*>(dst + j * 4) = acc[j];
    if (kg == 0) lds_l[t4 * NWAVE + wave] = l;
  }
  __syncthreads();
  {
    float o = 0.f;
#pragma unroll
    for (int w2 = 0; w2 < NWAVE; ++w2) o += lds_acc[w2 * 512 + tid];
    const int g = tid >> 7, ekg = (tid >> 5) & 3, j = (tid >> 2) & 7, r = tid & 3;
    const int dim = 32 * ekg + 8 * r + j;
    po[(((h * G + g) * NSPLIT) + s) * D + dim] = o;
    if (tid < 16 && c4 == 0) {
      const f32x4 a = *reinterpret_cast<const f32x4*>(lds_l + t4 * NWAVE);
      const f32x4 b = *reinterpret_cast<const f32x4*>(lds_l + t4 * NWAVE + 4);
      const float L = ((a[0] + a[1]) + (a[2] + a[3])) + ((b[0] + b[1]) + (b[2] + b[3]));
      *reinterpret_cast<float2*>(pml + (((h * G + t4) * NSPLIT) + s) * 2) = make_float2(m, L);
    }
  }
}

__global__ void __launch_bounds__(64) attn_combine(const float* __restrict__ po, __hip_bfloat16* __restrict__ out) {
  const float* pml = po + HQ * NSPLIT * D;
  const int hq = blockIdx.x, lane = threadIdx.x;
  float2 o[NSPLIT];
#pragma unroll
  for (int s = 0; s < NSPLIT; ++s) o[s] = *reinterpret_cast<const float2*>(po + ((size_t)hq * NSPLIT + s) * D + 2 * lane);
  const float2 ml = *reinterpret_cast<const float2*>(pml + ((size_t)hq * NSPLIT + (lane & (NSPLIT - 1))) * 2);
  __builtin_amdgcn_sched_barrier(0);
  float M = ml.x;
#pragma unroll
  for (int d = 1; d < NSPLIT; d <<= 1) M = fmaxf(M, __shfl_xor(M, d));
  const float wgt = (ml.y > 0.f) ? __builtin_amdgcn_exp2f(ml.x - M) : 0.f;
  float L = wgt * ml.y;
#pragma unroll
  for (int d = 1; d < NSPLIT; d <<= 1) L += __shfl_xor(L, d);
  float o0 = 0.f, o1 = 0.f;
#pragma unroll
  for (int s = 0; s < NSPLIT; ++s) {
    const float ws = __builtin_bit_cast(float, __builtin_amdgcn_readlane(__builtin_bit_cast(int, wgt), s));
    o0 = fmaf(ws, o[s].x, o0); o1 = fmaf(ws, o[s].y, o1);
  }
  const float inv = (L > 0.f) ? 1.f / L : 0.f;
  *reinterpret_cast<unsigned*>(out + hq * D + 2 * lane) = cvtpk(o0 * inv, o1 * inv);
}

extern "C" void kernel_launch(void* const* d_in, const int* in_sizes, int n_in, void* d_out, int out_size, void* d_ws, size_t ws_size, hipStream_t stream) {
  float* po = (float*)d_ws;
  attn_split<<<dim3(HKV * NSPLIT), dim3(NWAVE * 64), 0, stream>>>((const unsigned char*)d_in[0], (const unsigned char*)d_in[1], (const unsigned char*)d_in[2],
      (const int*)d_in[3], (const int*)d_in[4], po);
  attn_combine<<<dim3(HQ), dim3(64), 0, stream>>>(po, (__hip_bfloat16*)d_out);
}
```

```cpp
#include <hip/hip_runtime.h>
#include <hip/hip_bf16.h>
#include <stdint.h>
#define STAMP(k)
#define STAMPW(k, n)
#define STAMP_DECL
#define STAMP_STORE(base, wid)

typedef unsigned u32x4 __attribute__((ext_vector_type(4)));
typedef unsigned u32x2 __attribute__((ext_vector_type(2)));
typedef float f32x4 __attribute__((ext_vector_type(4)));
typedef short s16x4 __attribute__((ext_vector_type(4)));
typedef __bf16 bf16x8 __attribute__((ext_vector_type(8)));

#define KV_AUX 2
#define WIN 5
#define FUSED 1

constexpr int HQ = 32, HKV = 8, G = 4, D = 128, PAGE = 16, NBPS = 2048;
constexpr int NWAVE = 8, NWG = 256, NGRP = 4, NSPLIT = NWG / NGRP;
constexpr int SLOTS = 256;
constexpr int PPW = NBPS / SLOTS;
constexpr int ROWB = HKV * D * 2;
constexpr int PAGEB = PAGE * ROWB;
constexpr float C2 = 0.088388347648318440f * 1.4426950408889634f;
static_assert(PPW == 8 && 2 * WIN >= PPW, "prologue loads 8 block-table entries; two windows cover them");
constexpr unsigned WS_EA = 0, WS_GR = 512, GR_BYTES = NGRP * NSPLIT * 8 * 64 * 16, WS_EB = WS_GR + GR_BYTES, WS_END = WS_EB + 512;
constexpr unsigned TC1 = 0x9E3779B1u, TC2 = 0x85EBCA77u, TC3 = 0x27D4EB2Fu;
constexpr bool tag_ok() { for (unsigned b = 0; b < 256; ++b) { const unsigned p = b * 0x01010101u; if (p * TC1 + p * TC2 + TC3 == p) return false; } return TC1 + TC2 != 0u; }
static_assert(tag_ok(), "tag must differ from any constant-byte fill");

template <int CTRL> __device__ __forceinline__ float dpp(float x) {
  return __builtin_bit_cast(float, __builtin_amdgcn_mov_dpp(__builtin_bit_cast(int, x), CTRL, 0xf, 0xf, true));
}
template <int CTRL> __device__ __forceinline__ unsigned dppu(unsigned x) {
  return (unsigned)__builtin_amdgcn_mov_dpp((int)x, CTRL, 0xf, 0xf, true);
}
__device__ __forceinline__ float xrow16_max(float x) {
  auto s = __builtin_amdgcn_permlane16_swap(__float_as_uint(x), __float_as_uint(x), false, false);
  x = fmaxf(__uint_as_float(s[0]), __uint_as_float(s[1]));
  auto t = __builtin_amdgcn_permlane32_swap(__float_as_uint(x), __float_as_uint(x), false, false);
  return fmaxf(__uint_as_float(t[0]), __uint_as_float(t[1]));
}
__device__ __forceinline__ float xrow16_sum(float x) {
  auto s = __builtin_amdgcn_permlane16_swap(__float_as_uint(x), __float_as_uint(x), false, false);
  x = __uint_as_float(s[0]) + __uint_as_float(s[1]);
  auto t = __builtin_amdgcn_permlane32_swap(__float_as_uint(x), __float_as_uint(x), false, false);
  return __uint_as_float(t[0]) + __uint_as_float(t[1]);
}
__device__ __forceinline__ unsigned cvtpk(float lo, float hi) {
  typedef __bf16 bf16x2_t __attribute__((ext_vector_type(2)));
  typedef float f32x2_t __attribute__((ext_vector_type(2)));
  f32x2_t v = {lo, hi};
  return __builtin_bit_cast(unsigned, __builtin_convertvector(v, bf16x2_t));
}

__device__ __forceinline__ void combine_store(const u32x4 (&g)[8], int lane, unsigned* outp) {
  float M = -INFINITY;
#pragma unroll
  for (int k = 0; k < 8; ++k) M = fmaxf(M, __uint_as_float(g[k][1]));
  M = fmaxf(M, dpp<0x128>(M));
  M = xrow16_max(M);
  const float msub = (M == -INFINITY) ? 0.f : M;
  float W = 0.f, a0 = 0.f, a1 = 0.f;
#pragma unroll
  for (int k = 0; k < 8; ++k) {
    const float w = __builtin_amdgcn_exp2f(__uint_as_float(g[k][1]) - msub);
    W += w; a0 = fmaf(w, __uint_as_float(g[k][2]), a0); a1 = fmaf(w, __uint_as_float(g[k][3]), a1);
  }
  W += dpp<0x128>(W); a0 += dpp<0x128>(a0); a1 += dpp<0x128>(a1);
  W = xrow16_sum(W); a0 = xrow16_sum(a0); a1 = xrow16_sum(a1);
  const float inv = (W > 0.f) ? 1.f / W : 0.f;
  if (lane < 8) outp[lane] = cvtpk(a0 * inv, a1 * inv);
}

__global__ void __launch_bounds__(NWAVE * 64, 2)
attn_split(const unsigned char* __restrict__ q, const unsigned char* __restrict__ kc, const unsigned char* __restrict__ vc,
           const int* __restrict__ bt, const int* __restrict__ lens, unsigned char* __restrict__ ws, unsigned* __restrict__ out) {
  __shared__ __attribute__((aligned(16))) float lds[NWAVE * 512 + 2 * NWAVE * 4];
  STAMP_DECL; STAMP(0);
  float* lds_acc = lds;
  float* lds_ml = lds + NWAVE * 512;
  const int hp = blockIdx.x & 3, idx = blockIdx.x >> 2;
  const int tid = threadIdx.x, lane = tid & 63;
  const int wave = __builtin_amdgcn_readfirstlane(tid >> 6);
  const int h = 2 * hp + (wave & 1);
  const int slot = (wave >> 1) * NSPLIT + idx;
  const int kg = lane >> 4, t4 = (lane >> 2) & 3, c4 = lane & 3, c16 = lane & 15;
  const u32x4 qf = *reinterpret_cast<const u32x4*>(q + (h * G + t4) * (D * 2) + kg * 64 + c4 * 16);

  unsigned ph[PPW + 2], ea, eb;
  int len;
  {
    const int* btw = bt + slot;
    const unsigned char* ep = ws + hp * 128;
    asm volatile(
        "s_load_dword %0, %12, 0x0\n\t"
        "s_load_dword %1, %11, 0x0\n\t"
        "s_load_dword %2, %11, 0x400\n\t"
        "s_load_dword %3, %11, 0x800\n\t"
        "s_load_dword %4, %11, 0xc00\n\t"
        "s_load_dword %5, %11, 0x1000\n\t"
        "s_load_dword %6, %11, 0x1400\n\t"
        "s_load_dword %7, %11, 0x1800\n\t"
        "s_load_dword %8, %11, 0x1c00\n\t"
        "s_load_dword %9, %13, 0x0\n\t"
        "s_load_dword %10, %14, 0x0\n\t"
        "s_waitcnt lgkmcnt(0)"
        : "=&s"(len), "=&s"(ph[0]), "=&s"(ph[1]), "=&s"(ph[2]), "=&s"(ph[3]), "=&s"(ph[4]), "=&s"(ph[5]), "=&s"(ph[6]), "=&s"(ph[7]), "=&s"(ea), "=&s"(eb)
        : "s"(btw), "s"(lens), "s"(ep + WS_EA), "s"(ep + WS_EB));
    ph[8] = 0; ph[9] = 0;
  }
  STAMP(1);
  len = min(len, NBPS * PAGE);
  const int nwin = (len > SLOTS * WIN * PAGE) ? 2 : 1;

  const unsigned koffA = h * (D * 2) + t4 * 4 * ROWB + kg * 64 + c4 * 16, koffB = koffA + 2 * ROWB;
  const unsigned voffA = h * (D * 2) + kg * 4 * ROWB + c16 * 16, voffB = voffA + 2 * ROWB;

  f32x4 acc[8];
#pragma unroll
  for (int j = 0; j < 8; ++j) acc[j] = f32x4{0.f, 0.f, 0.f, 0.f};
  float m = -INFINITY, l = 0.f;

  int rem0 = len - slot * PAGE;
  int w = 0;
  do {
    u32x4 K[WIN][4], V[WIN][4];
#pragma unroll
    for (int i = 0; i < WIN; ++i) {
      const unsigned off = ph[i] * PAGEB;
      const unsigned nrec = off + (unsigned)max(rem0 - i * SLOTS * PAGE, 0) * ROWB;
      const __amdgpu_buffer_rsrc_t r = __builtin_amdgcn_make_buffer_rsrc((void*)kc, 0, nrec, 0x00020000);
      K[i][0] = __builtin_amdgcn_raw_buffer_load_b128(r, koffA, off, KV_AUX);
      K[i][1] = __builtin_amdgcn_raw_buffer_load_b128(r, koffA + ROWB, off, KV_AUX);
      K[i][2] = __builtin_amdgcn_raw_buffer_load_b128(r, koffB, off, KV_AUX);
      K[i][3] = __builtin_amdgcn_raw_buffer_load_b128(r, koffB + ROWB, off, KV_AUX);
    }
    __builtin_amdgcn_sched_barrier(0);
    STAMP(2);
    float S[WIN][4];
    float mx = -INFINITY;
#pragma unroll
    for (int i = 0; i < WIN; ++i) {
      const int remi = rem0 - i * SLOTS * PAGE - 4 * kg;
#pragma unroll
      for (int t = 0; t < 4; ++t) {
        f32x4 d = __builtin_amdgcn_mfma_f32_16x16x32_bf16(__builtin_bit_cast(bf16x8, K[i][t]), __builtin_bit_cast(bf16x8, qf), f32x4{0.f, 0.f, 0.f, 0.f}, 0, 0, 0);
        float x = dpp<0x00>(d[0]);
        x += dpp<0x55>(d[1]);
        x += dpp<0xAA>(d[2]);
        x += dpp<0xFF>(d[3]);
        x = (t < remi) ? x * C2 : -INFINITY;
        S[i][t] = x;
        mx = fmaxf(mx, x);
      }
    }
    mx = xrow16_max(mx);
    const float mn = fmaxf(m, mx);
    const float msub = (mn == -INFINITY) ? 0.f : mn;
    {
      const float alpha = __builtin_amdgcn_exp2f(m - msub);
      l *= alpha;
#pragma unroll
      for (int j = 0; j < 8; ++j) acc[j] *= alpha;
    }
    m = mn;
    u32x2 P[WIN];
#pragma unroll
    for (int i = 0; i < WIN; ++i) {
      float p[4];
#pragma unroll
      for (int t = 0; t < 4; ++t) { p[t] = __builtin_amdgcn_exp2f(S[i][t] - msub); l += p[t]; }
      P[i] = u32x2{cvtpk(p[0], p[1]), cvtpk(p[2], p[3])};
    }
    __builtin_amdgcn_sched_barrier(0);
    STAMP(3);
#pragma unroll
    for (int i = 0; i < WIN; ++i) {
      const unsigned off = ph[i] * PAGEB;
      const unsigned nrec = off + (unsigned)max(rem0 - i * SLOTS * PAGE, 0) * ROWB;
      const __amdgpu_buffer_rsrc_t r = __builtin_amdgcn_make_buffer_rsrc((void*)vc, 0, nrec, 0x00020000);
      V[i][0] = __builtin_amdgcn_raw_buffer_load_b128(r, voffA, off, KV_AUX);
      V[i][1] = __builtin_amdgcn_raw_buffer_load_b128(r, voffA + ROWB, off, KV_AUX);
      V[i][2] = __builtin_amdgcn_raw_buffer_load_b128(r, voffB, off, KV_AUX);
      V[i][3] = __builtin_amdgcn_raw_buffer_load_b128(r, voffB + ROWB, off, KV_AUX);
    }
    __builtin_amdgcn_sched_barrier(0);
    STAMP(4);
#pragma unroll
    for (int i = 0; i < WIN; ++i) {
#pragma unroll
      for (int wd = 0; wd < 4; ++wd) {
        const u32x2 lo = {__builtin_amdgcn_perm(V[i][1][wd], V[i][0][wd], 0x05040100u), __builtin_amdgcn_perm(V[i][3][wd], V[i][2][wd], 0x05040100u)};
        const u32x2 hi = {__builtin_amdgcn_perm(V[i][1][wd], V[i][0][wd], 0x07060302u), __builtin_amdgcn_perm(V[i][3][wd], V[i][2][wd], 0x07060302u)};
        acc[2 * wd] = __builtin_amdgcn_mfma_f32_16x16x16bf16_1k(__builtin_bit_cast(s16x4, lo), __builtin_bit_cast(s16x4, P[i]), acc[2 * wd], 0, 0, 0);
        acc[2 * wd + 1] = __builtin_amdgcn_mfma_f32_16x16x16bf16_1k(__builtin_bit_cast(s16x4, hi), __builtin_bit_cast(s16x4, P[i]), acc[2 * wd + 1], 0, 0, 0);
      }
      __builtin_amdgcn_sched_barrier(0);
    }
#pragma unroll
    for (int i = 0; i < WIN; ++i) ph[i] = ph[i + WIN];
    rem0 -= WIN * SLOTS * PAGE;
  } while (++w < nwin);
  STAMP(5);
  l = xrow16_sum(l);
  if (c4 == 0) {
    float* dst = lds_acc + wave * 512 + (t4 * 4 + kg) * 32;
#pragma unroll
    for (int j = 0; j < 8; ++j) *reinterpret_cast<f32x4*>(dst + j * 4) = acc[j];
    if (kg == 0) *reinterpret_cast<float2*>(lds_ml + (((wave & 1) * 4 + t4) * 4 + (wave >> 1)) * 2) = make_float2(m, l);
  }
  __syncthreads();
  u32x4 gr;
  {
    const int par = wave >> 2, g = wave & 3;
    const f32x4 a = *reinterpret_cast<const f32x4*>(lds_ml + wave * 8);
    const f32x4 b = *reinterpret_cast<const f32x4*>(lds_ml + wave * 8 + 4);
    const float* src = lds_acc + par * 512 + (g * 4 + kg) * 32 + (2 * c4) * 4 + t4;
    float x0[4], x1[4];
#pragma unroll
    for (int k = 0; k < 4; ++k) { x0[k] = src[k * 1024]; x1[k] = src[k * 1024 + 4]; }
    const float M = fmaxf(fmaxf(a[0], a[2]), fmaxf(b[0], b[2]));
    const float ms = (M == -INFINITY) ? 0.f : M;
    const float w0 = __builtin_amdgcn_exp2f(a[0] - ms), w1 = __builtin_amdgcn_exp2f(a[2] - ms), w2 = __builtin_amdgcn_exp2f(b[0] - ms), w3 = __builtin_amdgcn_exp2f(b[2] - ms);
    const float L = (w0 * a[1] + w1 * a[3]) + (w2 * b[1] + w3 * b[3]);
    const float inv = (L > 0.f) ? __builtin_amdgcn_rcpf(L) : 0.f;
    const float o0 = ((w0 * x0[0] + w1 * x0[1]) + (w2 * x0[2] + w3 * x0[3])) * inv;
    const float o1 = ((w0 * x1[0] + w1 * x1[1]) + (w2 * x1[2] + w3 * x1[3])) * inv;
    const float lse = M + __builtin_amdgcn_logf(L);
    const unsigned tag = ea * TC1 + eb * TC2 + TC3;
    gr = u32x4{tag, __float_as_uint(lse), __float_as_uint(o0), __float_as_uint(o1)};
  }
  const __amdgpu_buffer_rsrc_t wr = __builtin_amdgcn_make_buffer_rsrc((void*)ws, 0, WS_END, 0x00020000);
  __builtin_amdgcn_raw_buffer_store_b128(gr, wr, WS_GR + ((hp * NSPLIT + idx) * 8 + wave) * 1024 + lane * 16, 0, FUSED ? 16 : 0);
  STAMP(6);
  if (wave == 0) {
    const unsigned tag = gr[0];
    const int qh = idx >> 3, sl = idx & 7;
    const unsigned base = WS_GR + (hp * NSPLIT + (lane >> 3)) * 8192 + qh * 1024 + sl * 128 + (lane & 7) * 16;
    u32x4 g[8];
    for (unsigned spin = 0; spin < (1u << 22); ++spin) {
      bool ok = true;
#pragma unroll
      for (int k = 0; k < 8; ++k) g[k] = __builtin_amdgcn_raw_buffer_load_b128(wr, base, k * 8 * 8192, 16);
#pragma unroll
      for (int k = 0; k < 8; ++k) ok &= (g[k][0] == tag) & (g[k][1] == dppu<0xB1>(g[k][1]));
      if (__all(ok)) break;
      __builtin_amdgcn_s_sleep(1);
    }
    STAMP(7);
    combine_store(g, lane, out + ((hp * 8 + qh) * D + sl * 16) / 2);
    if (idx == 0 && lane == 0) {
      __hip_atomic_store((unsigned*)(ws + WS_EA + hp * 128), ea + 1u, __ATOMIC_RELAXED, __HIP_MEMORY_SCOPE_AGENT);
      __hip_atomic_store((unsigned*)(ws + WS_EB + hp * 128), eb + 1u, __ATOMIC_RELAXED, __HIP_MEMORY_SCOPE_AGENT);
    }
    STAMPW(8, 0);
  }
  STAMP_STORE(ws, blockIdx.x * NWAVE + wave);
}

extern "C" void kernel_launch(void* const* d_in, const int* in_sizes, int n_in, void* d_out, int out_size, void* d_ws, size_t ws_size, hipStream_t stream) {
  attn_split<<<dim3(NWG), dim3(NWAVE * 64), 0, stream>>>((const unsigned char*)d_in[0], (const unsigned char*)d_in[1], (const unsigned char*)d_in[2],
      (const int*)d_in[3], (const int*)d_in[4], (unsigned char*)d_ws, (unsigned*)d_out);
}
```

```cpp
#include <hip/hip_runtime.h>
#include <hip/hip_bf16.h>
#include <stdint.h>
#include <type_traits>

typedef unsigned u32x16 __attribute__((ext_vector_type(16)));
typedef unsigned u32x4 __attribute__((ext_vector_type(4)));
typedef unsigned u32x2 __attribute__((ext_vector_type(2)));
typedef float f32x4 __attribute__((ext_vector_type(4)));
typedef float f32x2 __attribute__((ext_vector_type(2)));
typedef short s16x4 __attribute__((ext_vector_type(4)));
typedef __bf16 bf16x8 __attribute__((ext_vector_type(8)));
typedef __attribute__((address_space(1))) unsigned gu32;

constexpr int NT = 2, SC1 = 16;
constexpr int HQ = 32, HKV = 8, G = 4, D = 128, PAGE = 16, NBPS = 2048;
constexpr int NWAVE = 8, NWG = 256, NGRP = 4, NSPLIT = NWG / NGRP;
constexpr int SLOTS = 256;
constexpr int WIN = 5;
constexpr int PPW = NBPS / SLOTS;
constexpr int ROWB = HKV * D * 2;
constexpr int PAGEB = PAGE * ROWB;
constexpr float C2 = 0.088388347648318440f * 1.4426950408889634f;
static_assert(PPW == 8 && 2 * (WIN - 1) == PPW, "the prologue loads 8 block-table entries; two windows cover them");
constexpr unsigned WS_EA = 0, WS_GR = 512, GR_BYTES = NGRP * NSPLIT * 8 * 512, WS_EB = WS_GR + GR_BYTES, WS_END = WS_EB + 512;
constexpr unsigned TC1 = 0x9E3779B1u, TC2 = 0x85EBCA77u, TC3 = 0x27D4EB2Fu;
constexpr bool tag_ok() {
  for (unsigned b = 0; b < 256; ++b) { const unsigned p = b * 0x01010101u; if (p * TC1 + p * TC2 + TC3 == p) return false; }
  return TC1 + TC2 != 0u;
}
static_assert(tag_ok(), "the tag must differ from any constant-byte fill and change with either epoch word");

template <int CTRL> __device__ __forceinline__ float dpp(float x) {
  return __builtin_bit_cast(float, __builtin_amdgcn_mov_dpp(__builtin_bit_cast(int, x), CTRL, 0xf, 0xf, true));
}
template <int CTRL> __device__ __forceinline__ unsigned dppu(unsigned x) {
  return (unsigned)__builtin_amdgcn_mov_dpp((int)x, CTRL, 0xf, 0xf, true);
}
__device__ __forceinline__ float xrow16_max(float x) {
  auto s = __builtin_amdgcn_permlane16_swap(__float_as_uint(x), __float_as_uint(x), false, false);
  x = fmaxf(__uint_as_float(s[0]), __uint_as_float(s[1]));
  auto t = __builtin_amdgcn_permlane32_swap(__float_as_uint(x), __float_as_uint(x), false, false);
  return fmaxf(__uint_as_float(t[0]), __uint_as_float(t[1]));
}
__device__ __forceinline__ float xrow16_sum(float x) {
  auto s = __builtin_amdgcn_permlane16_swap(__float_as_uint(x), __float_as_uint(x), false, false);
  x = __uint_as_float(s[0]) + __uint_as_float(s[1]);
  auto t = __builtin_amdgcn_permlane32_swap(__float_as_uint(x), __float_as_uint(x), false, false);
  return __uint_as_float(t[0]) + __uint_as_float(t[1]);
}
__device__ __forceinline__ unsigned cvtpk(float lo, float hi) {
  typedef __bf16 bf16x2_t __attribute__((ext_vector_type(2)));
  f32x2 v = {lo, hi};
  return __builtin_bit_cast(unsigned, __builtin_convertvector(v, bf16x2_t));
}
__device__ __forceinline__ const unsigned char* mkptr(unsigned lo, unsigned hi) { return (const unsigned char*)(((unsigned long long)hi << 32) | lo); }
__device__ __forceinline__ __amdgpu_buffer_rsrc_t srd(const void* p, unsigned bytes) { return __builtin_amdgcn_make_buffer_rsrc((void*)p, 0, bytes, 0x00020000); }

__global__ void __launch_bounds__(NWAVE * 64, 2)
attn_decode(const void* a_q, const void* a_k, const void* a_v, const void* a_bt, const void* a_lens, void* a_ws, void* a_out, void* a_pad) {
  __shared__ __attribute__((aligned(16))) float lds[NWAVE * 512 + 2 * NWAVE * 4];
  u32x16 ka;
  asm volatile("s_load_dwordx16 %0, %1, 0x0" : "=s"(ka) : "s"(__builtin_amdgcn_kernarg_segment_ptr()));
  __builtin_amdgcn_s_setprio(3);
  float* lds_acc = lds;
  float* lds_ml = lds + NWAVE * 512;
  const int hp = blockIdx.x & 3, idx = blockIdx.x >> 2;
  const int tid = threadIdx.x, lane = tid & 63;
  const int wave = __builtin_amdgcn_readfirstlane(tid >> 6);
  const int h = 2 * hp + (wave & 1);
  const int slot = (wave >> 1) * NSPLIT + idx;
  const int kg = lane >> 4, t4 = (lane >> 2) & 3, c4 = lane & 3, c16 = lane & 15;
  const unsigned koffA = h * (D * 2) + t4 * 4 * ROWB + kg * 64 + c4 * 16, koffB = koffA + 2 * ROWB;
  const unsigned qoff = (h * G + t4) * (D * 2) + kg * 64 + c4 * 16;
  const unsigned slot4 = slot * 4;
  const unsigned goff = WS_GR + hp * NSPLIT * 4096 + (idx >> 3) * 512 + (idx & 7) * 64 + c4 * 16;
  const unsigned preoff = (goff + (((lane >> 2) & 7) * 8 + wave) * 4096) | ((unsigned)(lane & 32) << 26);
  const unsigned wj = (blockIdx.x >> 3) * NWAVE + wave;
  const unsigned woff = ((wj & 31) << 21) + (wj >> 6) * 4096 + (blockIdx.x & 7) * 128;
  asm volatile("s_waitcnt lgkmcnt(0)" : "+s"(ka) : "s"(slot4), "v"(koffA), "v"(koffB), "v"(qoff), "s"(woff), "v"(preoff));
  const unsigned char* q = mkptr(ka[0], ka[1]);
  const unsigned char* kc = mkptr(ka[2], ka[3]);
  const unsigned char* vc = mkptr(ka[4], ka[5]);
  const unsigned char* lens = mkptr(ka[8], ka[9]);
  unsigned char* ws = (unsigned char*)mkptr(ka[10], ka[11]);
  const unsigned char* out = mkptr(ka[12], ka[13]);
  unsigned ph[PPW];
  int len;
  {
    const unsigned char* btw = mkptr(ka[6], ka[7]) + slot4;
    asm volatile(
        "s_load_dword %0, %10, 0x0\n\t"
        "s_load_dword %1, %9, 0x0\n\t"
        "s_load_dword %2, %9, 0x400\n\t"
        "s_load_dword %3, %9, 0x800\n\t"
        "s_load_dword %4, %9, 0xc00\n\t"
        "s_load_dword %5, %9, 0x1000\n\t"
        "s_load_dword %6, %9, 0x1400\n\t"
        "s_load_dword %7, %9, 0x1800\n\t"
        "s_load_dword %8, %9, 0x1c00"
        : "=&s"(len), "=&s"(ph[0]), "=&s"(ph[1]), "=&s"(ph[2]), "=&s"(ph[3]), "=&s"(ph[4]), "=&s"(ph[5]), "=&s"(ph[6]), "=&s"(ph[7])
        : "s"(btw), "s"(lens));
  }
  __builtin_amdgcn_sched_barrier(0);
  const unsigned warm = __builtin_amdgcn_raw_buffer_load_b32(srd((wj & 32) ? vc : kc, NBPS * PAGEB), 0, woff, NT);
  const u32x4 qf = __builtin_amdgcn_raw_buffer_load_b128(srd(q, HQ * D * 2), qoff, 0, 0);
  const __amdgpu_buffer_rsrc_t wr = srd(ws, WS_END);
  const unsigned ea = __builtin_amdgcn_raw_buffer_load_b32(wr, 0, WS_EA + hp * 128, 0);
  const unsigned eb = __builtin_amdgcn_raw_buffer_load_b32(wr, 0, WS_EB + hp * 128, 0);
  const unsigned pre = __builtin_amdgcn_raw_buffer_load_b32(wr, preoff, 0, SC1);
  const unsigned pre2 = __builtin_amdgcn_raw_buffer_load_b32(srd(out, HQ * D * 2), 0, (hp * NSPLIT + idx) * 32, SC1);
  __builtin_amdgcn_sched_barrier(0);
  asm volatile("s_waitcnt lgkmcnt(0)" : "+s"(len), "+s"(ph[0]), "+s"(ph[1]), "+s"(ph[2]), "+s"(ph[3]), "+s"(ph[4]), "+s"(ph[5]), "+s"(ph[6]), "+s"(ph[7]));
  const int lenl = ((unsigned)(len - 1) < (unsigned)(NBPS * PAGE)) ? len : NBPS * PAGE;
  const unsigned kfull = len <= 0 ? 0u : (unsigned)PAGEB;
  const int nfull = lenl >> 12;
  unsigned pr[4];
  u32x4 K[WIN][4];
  auto load_k = [&](auto basec) __attribute__((always_inline)) {
    constexpr int PB = decltype(basec)::value;
#pragma unroll
    for (int i = 0; i < WIN - 1; ++i) {
      const unsigned off = ph[PB + i] * PAGEB;
      const __amdgpu_buffer_rsrc_t r = srd(kc, (PB + i < nfull) ? off + kfull : 0u);
      K[i][0] = __builtin_amdgcn_raw_buffer_load_b128(r, koffA, off, NT);
      K[i][1] = __builtin_amdgcn_raw_buffer_load_b128(r, koffA + ROWB, off, NT);
      K[i][2] = __builtin_amdgcn_raw_buffer_load_b128(r, koffB, off, NT);
      K[i][3] = __builtin_amdgcn_raw_buffer_load_b128(r, koffB + ROWB, off, NT);
      __builtin_amdgcn_sched_barrier(0);
      if (PB == 0 && i == 0) {
        const unsigned char* btr = mkptr(ka[6], ka[7]) + (min(nfull, PPW - 1) * SLOTS + (slot >> 2)) * 4;
        asm volatile("s_load_dword %0, %4, 0x0\n\ts_load_dword %1, %4, 0x100\n\ts_load_dword %2, %4, 0x200\n\ts_load_dword %3, %4, 0x300"
                     : "=&s"(pr[0]), "=&s"(pr[1]), "=&s"(pr[2]), "=&s"(pr[3]) : "s"(btr));
        __builtin_amdgcn_sched_barrier(0);
      }
    }
  };
  __builtin_amdgcn_sched_barrier(0);
  load_k(std::integral_constant<int, 0>{});
  asm volatile("s_waitcnt lgkmcnt(0)" : "+s"(pr[0]), "+s"(pr[1]), "+s"(pr[2]), "+s"(pr[3]));
  const int remq = (lenl & 4095) - (slot >> 2) * PAGE;
  const unsigned voffA = h * (D * 2) + kg * 4 * ROWB + c16 * 16, voffB = voffA + 2 * ROWB;

  f32x4 accE = {0.f, 0.f, 0.f, 0.f}, accO = {0.f, 0.f, 0.f, 0.f};
  float m = -INFINITY, l = 0.f;
  float mw0, mw1, mw2, mw3, lse;
  unsigned tag, poff, gbase, srcoff, ooff;

  auto window = [&](auto lastc, auto basec) __attribute__((always_inline)) {
    constexpr bool LAST = decltype(lastc)::value;
    constexpr int PB = decltype(basec)::value;
    constexpr int NS = LAST ? WIN : WIN - 1;
    u32x4 V[WIN][4];
    if (PB != 0) load_k(basec);
    if (LAST) {
#pragma unroll
      for (int t = 0; t < 4; ++t) {
        const unsigned off = pr[t] * PAGEB;
        K[WIN - 1][t] = __builtin_amdgcn_raw_buffer_load_b128(srd(kc, off + (unsigned)max(remq - t * 64 * PAGE, 0) * ROWB), koffA, off + (slot & 3) * ROWB, NT);
      }
      __builtin_amdgcn_sched_barrier(0);
    }
    __builtin_amdgcn_s_setprio(0);
    __builtin_amdgcn_sched_barrier(0);
    float S[WIN][4];
    float mx = -INFINITY;
#pragma unroll
    for (int i = 0; i < NS; ++i) {
      const float bias = (PB + i < nfull) ? 0.f : -INFINITY;
#pragma unroll
      for (int t = 0; t < 4; ++t) {
        f32x4 d = __builtin_amdgcn_mfma_f32_16x16x32_bf16(__builtin_bit_cast(bf16x8, K[i][t]), __builtin_bit_cast(bf16x8, qf), f32x4{0.f, 0.f, 0.f, 0.f}, 0, 0, 0);
        float x = dpp<0x00>(d[0]);
        x += dpp<0x55>(d[1]);
        x += dpp<0xAA>(d[2]);
        x += dpp<0xFF>(d[3]);
        if (i < WIN - 1) x = fmaf(x, C2, bias);
        else x = (4 * kg + (slot & 3) < remq - t * 64 * PAGE) ? x * C2 : -INFINITY;
        S[i][t] = x;
        mx = fmaxf(mx, x);
      }
    }
    mx = xrow16_max(mx);
    const float mn = fmaxf(m, mx);
    const float msub = (mn == -INFINITY) ? 0.f : mn;
    {
      const float alpha = __builtin_amdgcn_exp2f(m - msub);
      l *= alpha; accE *= alpha; accO *= alpha;
    }
    m = mn;
    u32x2 P[WIN][4];
#pragma unroll
    for (int i = 0; i < NS; ++i) {
      float p[4];
#pragma unroll
      for (int t = 0; t < 4; ++t) { p[t] = __builtin_amdgcn_exp2f(S[i][t] - msub); l += p[t]; }
      const unsigned p01 = cvtpk(p[0], p[1]), p23 = cvtpk(p[2], p[3]);
#pragma unroll
      for (int jj = 0; jj < 4; ++jj) P[i][jj] = u32x2{c4 == jj ? p01 : 0u, c4 == jj ? p23 : 0u};
    }
    if (LAST) {
      const float lt = xrow16_sum(l);
      if (lane < 16 && c4 == 0) *reinterpret_cast<float2*>(lds_ml + (((wave & 1) * 4 + t4) * 4 + (wave >> 1)) * 2) = make_float2(m, lt);
    }
    __builtin_amdgcn_sched_barrier(0);
    if (LAST) {
#pragma unroll
      for (int t = 0; t < 4; ++t) {
        const unsigned off = pr[t] * PAGEB;
        V[WIN - 1][t] = __builtin_amdgcn_raw_buffer_load_b128(srd(vc, off + (unsigned)max(remq - t * 64 * PAGE, 0) * ROWB), voffA, off + (slot & 3) * ROWB, NT);
      }
    }
#pragma unroll
    for (int i = 0; i < WIN - 1; ++i) {
      const unsigned off = ph[PB + i] * PAGEB;
      const __amdgpu_buffer_rsrc_t r = srd(vc, (PB + i < nfull) ? off + PAGEB : 0u);
      V[i][0] = __builtin_amdgcn_raw_buffer_load_b128(r, voffA, off, NT);
      V[i][1] = __builtin_amdgcn_raw_buffer_load_b128(r, voffA + ROWB, off, NT);
      V[i][2] = __builtin_amdgcn_raw_buffer_load_b128(r, voffB, off, NT);
      V[i][3] = __builtin_amdgcn_raw_buffer_load_b128(r, voffB + ROWB, off, NT);
    }
    __builtin_amdgcn_sched_barrier(0);
    if (LAST) {
      __syncthreads();
      const f32x4 a = *reinterpret_cast<const f32x4*>(lds_ml + wave * 8);
      const f32x4 b = *reinterpret_cast<const f32x4*>(lds_ml + wave * 8 + 4);
      const float M = fmaxf(fmaxf(a[0], a[2]), fmaxf(b[0], b[2]));
      const float ms = (M == -INFINITY) ? 0.f : M;
      const float w0 = __builtin_amdgcn_exp2f(a[0] - ms), w1 = __builtin_amdgcn_exp2f(a[2] - ms), w2 = __builtin_amdgcn_exp2f(b[0] - ms), w3 = __builtin_amdgcn_exp2f(b[2] - ms);
      const float L = (w0 * a[1] + w1 * a[3]) + (w2 * b[1] + w3 * b[3]);
      const float inv = (L > 0.f) ? __builtin_amdgcn_rcpf(L) : 0.f;
      mw0 = w0 * inv; mw1 = w1 * inv; mw2 = w2 * inv; mw3 = w3 * inv;
      lse = M + __builtin_amdgcn_logf(L);
      tag = ea * TC1 + eb * TC2 + TC3;
      poff = (WS_GR + ((hp * NSPLIT + idx) * 8 + wave) * 512 + lane * 8) | ((unsigned)lane << 31);
      gbase = goff + (lane >> 2) * 4 * 4096;
      srcoff = (wave >> 2) * 512 + (kg * 16 + (wave & 3) * 4 + c4) * 4 + t4;
      ooff = (((hp * 8 + (idx >> 3)) * D + (idx & 7) * 16 + c4 * 4 + (kg & 1) * 2 + (kg >> 1)) * 2) | ((unsigned)t4 << 30);
      asm volatile("" : "+v"(mw0), "+v"(mw1), "+v"(mw2), "+v"(mw3), "+v"(lse), "+v"(tag), "+v"(poff), "+v"(gbase), "+v"(srcoff), "+v"(ooff) : "v"(warm), "v"(pre), "v"(pre2));
    }
    __builtin_amdgcn_sched_barrier(0);
#pragma unroll
    for (int ii = 0; ii < NS; ++ii) {
      const int i = LAST ? (ii + WIN - 1) % WIN : ii;
      unsigned lo01[4], hi01[4];
#pragma unroll
      for (int wd = 0; wd < 4; ++wd) { lo01[wd] = __builtin_amdgcn_perm(V[i][1][wd], V[i][0][wd], 0x05040100u); hi01[wd] = __builtin_amdgcn_perm(V[i][1][wd], V[i][0][wd], 0x07060302u); }
      __builtin_amdgcn_sched_barrier(0);
#pragma unroll
      for (int wd = 0; wd < 4; ++wd) {
        const u32x2 lo = {lo01[wd], __builtin_amdgcn_perm(V[i][3][wd], V[i][2][wd], 0x05040100u)};
        const u32x2 hi = {hi01[wd], __builtin_amdgcn_perm(V[i][3][wd], V[i][2][wd], 0x07060302u)};
        accE = __builtin_amdgcn_mfma_f32_16x16x16bf16_1k(__builtin_bit_cast(s16x4, lo), __builtin_bit_cast(s16x4, P[i][wd]), accE, 0, 0, 0);
        accO = __builtin_amdgcn_mfma_f32_16x16x16bf16_1k(__builtin_bit_cast(s16x4, hi), __builtin_bit_cast(s16x4, P[i][wd]), accO, 0, 0, 0);
        __builtin_amdgcn_sched_barrier(0);
      }
    }
  };

  auto finish = [&]() __attribute__((always_inline)) {
    *reinterpret_cast<f32x4*>(lds_acc + wave * 512 + lane * 4) = accE;
    *reinterpret_cast<f32x4*>(lds_acc + wave * 512 + 256 + lane * 4) = accO;
    const float* src = lds_acc + srcoff;
    __syncthreads();
    {
      f32x2 x[4];
#pragma unroll
      for (int k = 0; k < 4; ++k) x[k] = f32x2{src[k * 1024], src[k * 1024 + 256]};
      f32x2 o = x[0] * mw0;
      o = __builtin_elementwise_fma(x[1], f32x2{mw1, mw1}, o);
      o = __builtin_elementwise_fma(x[2], f32x2{mw2, mw2}, o);
      o = __builtin_elementwise_fma(x[3], f32x2{mw3, mw3}, o);
      const unsigned pk = cvtpk(o[0], o[1]);
      const unsigned pk2 = dppu<0xB1>(pk);
      __builtin_amdgcn_raw_buffer_store_b128(u32x4{(unsigned)__builtin_amdgcn_bitop3_b32(tag, pk, pk2, 0x96), __float_as_uint(lse), pk, pk2}, wr, poff, 0, SC1);
    }
    if (wave == 0) {
      const __amdgpu_buffer_rsrc_t orr = srd(out, HQ * D * 2);
      unsigned long long p0, p1, p2, p3;
      unsigned cnt;
#define QP " quad_perm:[1,0,3,2] row_mask:0xf bank_mask:0xf bound_ctrl:1\n\t"
#define RR4 " row_ror:4 row_mask:0xf bank_mask:0xf bound_ctrl:1\n\t"
#define RR8 " row_ror:8 row_mask:0xf bank_mask:0xf bound_ctrl:1\n\t"
      asm volatile(
          "s_mov_b64 %0, -1\n\ts_mov_b64 %1, 0\n\ts_mov_b64 %2, 0\n\ts_mov_b64 %3, 0\n\t"
          "v_mbcnt_lo_u32_b32 v205, -1, 0\n\tv_mbcnt_hi_u32_b32 v205, -1, v205\n\tv_mov_b32 v209, v205\n\tv_mov_b32 v213, v205\n\t"
          "s_sleep 8\n\t"
          "s_mov_b32 %4, 0x200000\n"
          ".Lpoll%=:\n\t"
          "s_mov_b64 exec, %0\n\tbuffer_load_dwordx4 v[200:203], %6, %7, 0 offen sc1\n\t"
          "s_mov_b64 exec, %1\n\tbuffer_load_dwordx4 v[204:207], %6, %7, %10 offen sc1\n\t"
          "s_mov_b64 exec, %2\n\tbuffer_load_dwordx4 v[208:211], %6, %7, %11 offen sc1\n\t"
          "s_mov_b64 exec, %3\n\tbuffer_load_dwordx4 v[212:215], %6, %7, %12 offen sc1\n\t"
          "s_mov_b64 exec, -1\n\t"
          "s_waitcnt vmcnt(0)\n\t"
          "v_max3_f32 v221, v201, v205, %13\n\t"
          "v_max3_f32 v221, v221, v209, v213\n\t"
          "v_xor_b32_dpp v216, v201, v201" QP
          "v_xor_b32_dpp v217, v205, v205" QP
          "v_max_f32_dpp v221, v221, v221" RR4
          "v_xor_b32_dpp v218, v209, v209" QP
          "v_xor_b32_dpp v219, v213, v213" QP
          "v_max_f32_dpp v221, v221, v221" RR8
          "v_bitop3_b32 v224, v200, v202, v203 bitop3:0x96\n\t"
          "v_mov_b32 v222, v221\n\t"
          "v_bitop3_b32 v225, v204, v206, v207 bitop3:0x96\n\t"
          "v_bitop3_b32 v226, v208, v210, v211 bitop3:0x96\n\t"
          "v_permlane16_swap_b32 v221, v222\n\t"
          "v_bitop3_b32 v227, v212, v214, v215 bitop3:0x96\n\t"
          "v_max_f32 v221, v221, v222\n\t"
          "v_bitop3_b32 v216, v224, v216, %5 bitop3:0xde\n\t"
          "v_mov_b32 v222, v221\n\t"
          "v_bitop3_b32 v217, v225, v217, %5 bitop3:0xde\n\t"
          "v_bitop3_b32 v218, v226, v218, %5 bitop3:0xde\n\t"
          "v_permlane32_swap_b32 v221, v222\n\t"
          "v_bitop3_b32 v219, v227, v219, %5 bitop3:0xde\n\t"
          "v_or3_b32 v220, v216, v217, v218\n\t"
          "v_or_b32 v220, v220, v219\n\t"
          "v_cmp_ne_u32 vcc, 0, v220\n\t"
          "s_cbranch_vccz .Ldone%=\n\t"
          "v_or_b32_dpp v216, v216, v216" QP
          "v_or_b32_dpp v217, v217, v217" QP
          "v_or_b32_dpp v218, v218, v218" QP
          "v_or_b32_dpp v219, v219, v219" QP
          "v_cmp_ne_u32 %0, 0, v216\n\t"
          "v_cmp_ne_u32 %1, 0, v217\n\t"
          "v_cmp_ne_u32 %2, 0, v218\n\t"
          "v_cmp_ne_u32 %3, 0, v219\n\t"
          "s_bcnt1_i32_b64 vcc_lo, %0\n\t"
          "s_cmp_gt_u32 vcc_lo, 32\n\t"
          "s_cselect_b64 %1, 0, %1\n\ts_cselect_b64 %2, 0, %2\n\ts_cselect_b64 %3, 0, %3\n\t"
          "s_cbranch_scc0 .Lns%=\n\t"
          "s_sleep 6\n"
          ".Lns%=:\n\t"
          "s_sub_u32 %4, %4, 1\n\t"
          "s_cbranch_scc0 .Lpoll%=\n"
          ".Ldone%=:\n\t"
          "v_max_f32 v221, v221, v222\n\t"
          "v_lshlrev_b32 v224, 16, v202\n\t"
          "v_sub_f32 v201, v201, v221\n\t"
          "v_sub_f32 v205, v205, v221\n\t"
          "v_sub_f32 v209, v209, v221\n\t"
          "v_sub_f32 v213, v213, v221\n\t"
          "v_exp_f32 v200, v201\n\t"
          "v_exp_f32 v204, v205\n\t"
          "v_exp_f32 v208, v209\n\t"
          "v_exp_f32 v212, v213\n\t"
          "v_and_b32 v225, %14, v202\n\t"
          "v_lshlrev_b32 v226, 16, v203\n\t"
          "v_and_b32 v227, %14, v203\n\t"
          "v_lshlrev_b32 v228, 16, v206\n\t"
          "v_and_b32 v229, %14, v206\n\t"
          "v_pk_mul_f32 v[224:225], v[224:225], v[200:201] op_sel_hi:[1,0]\n\t"
          "v_pk_mul_f32 v[226:227], v[226:227], v[200:201] op_sel_hi:[1,0]\n\t"
          "v_lshlrev_b32 v230, 16, v207\n\t"
          "v_and_b32 v231, %14, v207\n\t"
          "v_add_f32 v223, v200, v204\n\t"
          "v_pk_fma_f32 v[224:225], v[228:229], v[204:205], v[224:225] op_sel_hi:[1,0,1]\n\t"
          "v_pk_fma_f32 v[226:227], v[230:231], v[204:205], v[226:227] op_sel_hi:[1,0,1]\n\t"
          "v_lshlrev_b32 v228, 16, v210\n\t"
          "v_and_b32 v229, %14, v210\n\t"
          "v_lshlrev_b32 v230, 16, v211\n\t"
          "v_and_b32 v231, %14, v211\n\t"
          "v_add_f32 v222, v208, v212\n\t"
          "v_pk_fma_f32 v[224:225], v[228:229], v[208:209], v[224:225] op_sel_hi:[1,0,1]\n\t"
          "v_pk_fma_f32 v[226:227], v[230:231], v[208:209], v[226:227] op_sel_hi:[1,0,1]\n\t"
          "v_add_f32 v223, v223, v222\n\t"
          "v_lshlrev_b32 v228, 16, v214\n\t"
          "v_and_b32 v229, %14, v214\n\t"
          "v_add_f32_dpp v223, v223, v223" RR4
          "v_lshlrev_b32 v230, 16, v215\n\t"
          "v_and_b32 v231, %14, v215\n\t"
          "v_add_f32_dpp v223, v223, v223" RR8
          "v_pk_fma_f32 v[224:225], v[228:229], v[212:213], v[224:225] op_sel_hi:[1,0,1]\n\t"
          "v_pk_fma_f32 v[226:227], v[230:231], v[212:213], v[226:227] op_sel_hi:[1,0,1]\n\t"
          "v_mov_b32 v222, v223\n\t"
          "s_nop 0\n\t"
          "v_permlane32_swap_b32 v224, v225\n\t"
          "v_permlane32_swap_b32 v226, v227\n\t"
          "v_permlane16_swap_b32 v223, v222\n\t"
          "v_add_f32 v224, v224, v225\n\t"
          "v_add_f32 v226, v226, v227\n\t"
          "v_add_f32 v223, v223, v222\n\t"
          "s_nop 0\n\t"
          "v_mov_b32 v222, v223\n\t"
          "v_permlane16_swap_b32 v224, v226\n\t"
          "s_nop 0\n\t"
          "v_permlane32_swap_b32 v223, v222\n\t"
          "v_add_f32 v224, v224, v226\n\t"
          "v_add_f32 v223, v223, v222\n\t"
          "v_rcp_f32 v222, v223\n\t"
          "v_add_f32_dpp v224, v224, v224" RR4
          "v_cmp_lt_f32 vcc, 0, v223\n\t"
          "s_nop 0\n\t"
          "v_add_f32_dpp v224, v224, v224" RR8
          "v_cndmask_b32 v222, 0, v222, vcc\n\t"
          "v_mul_f32 v224, v224, v222\n\t"
          "v_cvt_pk_bf16_f32 v224, v224, v224\n\t"
          "buffer_store_short v224, %8, %9, 0 offen sc1"
          : "=&s"(p0), "=&s"(p1), "=&s"(p2), "=&s"(p3), "=&s"(cnt)
          : "v"(tag), "v"(gbase), "s"(wr), "v"(ooff), "s"(orr), "s"(3 * 4096), "s"(1 * 4096), "s"(2 * 4096), "s"(-3.0e38f), "s"(0xffff0000u)
          : "memory", "vcc", "scc", "v200", "v201", "v202", "v203", "v204", "v205", "v206", "v207", "v208", "v209", "v210", "v211", "v212", "v213", "v214", "v215",
            "v216", "v217", "v218", "v219", "v220", "v221", "v222", "v223", "v224", "v225", "v226", "v227", "v228", "v229", "v230", "v231");
      if (idx == 0 && tid == 0) {
        __hip_atomic_store((gu32*)(ws + WS_EA + hp * 128), ea + 1u, __ATOMIC_RELAXED, __HIP_MEMORY_SCOPE_AGENT);
        __hip_atomic_store((gu32*)(ws + WS_EB + hp * 128), eb + 1u, __ATOMIC_RELAXED, __HIP_MEMORY_SCOPE_AGENT);
      }
    }
  };

  if (__builtin_expect(nfull <= WIN - 1, 1)) {
    window(std::true_type{}, std::integral_constant<int, 0>{});
    finish();
  } else {
    window(std::false_type{}, std::integral_constant<int, 0>{});
    __builtin_amdgcn_s_setprio(3);
    window(std::true_type{}, std::integral_constant<int, WIN - 1>{});
    finish();
  }
}

extern "C" void kernel_launch(void* const* d_in, const int* in_sizes, int n_in, void* d_out, int out_size, void* d_ws, size_t ws_size, hipStream_t stream) {
  attn_decode<<<dim3(NWG), dim3(NWAVE * 64), 0, stream>>>(d_in[0], d_in[1], d_in[2], d_in[3], d_in[4], d_ws, d_out, d_ws);
}
```

```cpp
#include <hip/hip_runtime.h>
#include <hip/hip_bf16.h>
#include <stdint.h>
#include <type_traits>

typedef unsigned u32x16 __attribute__((ext_vector_type(16)));
typedef unsigned u32x4 __attribute__((ext_vector_type(4)));
typedef unsigned u32x2 __attribute__((ext_vector_type(2)));
typedef float f32x4 __attribute__((ext_vector_type(4)));
typedef float f32x2 __attribute__((ext_vector_type(2)));
typedef short s16x4 __attribute__((ext_vector_type(4)));
typedef __bf16 bf16x8 __attribute__((ext_vector_type(8)));
typedef __attribute__((address_space(1))) unsigned gu32;

constexpr int NT = 2, SC1 = 16;
constexpr int HQ = 32, HKV = 8, G = 4, D = 128, PAGE = 16, NBPS = 2048;
constexpr int NWAVE = 8, NWG = 256, NGRP = 4, NSPLIT = NWG / NGRP;
constexpr int SLOTS = 256;
constexpr int WIN = 5;
constexpr int PPW = NBPS / SLOTS;
constexpr int ROWB = HKV * D * 2;
constexpr int PAGEB = PAGE * ROWB;
constexpr float C2 = 0.088388347648318440f * 1.4426950408889634f;
static_assert(PPW == 8 && 2 * (WIN - 1) == PPW, "the prologue loads 8 block-table entries; two windows cover them");
constexpr unsigned WS_EA = 0, WS_GR = 512, GR_BYTES = NGRP * NSPLIT * 8 * 512, WS_EB = WS_GR + GR_BYTES, WS_END = WS_EB + 512;
constexpr unsigned TC1 = 0x9E3779B1u, TC2 = 0x85EBCA77u, TC3 = 0x27D4EB2Fu;
constexpr bool tag_ok() {
  for (unsigned b = 0; b < 256; ++b) { const unsigned p = b * 0x01010101u; if (p * TC1 + p * TC2 + TC3 == p) return false; }
  return TC1 + TC2 != 0u;
}
static_assert(tag_ok(), "the tag must differ from any constant-byte fill and change with either epoch word");

template <int CTRL> __device__ __forceinline__ float dpp(float x) {
  return __builtin_bit_cast(float, __builtin_amdgcn_mov_dpp(__builtin_bit_cast(int, x), CTRL, 0xf, 0xf, true));
}
template <int CTRL> __device__ __forceinline__ unsigned dppu(unsigned x) {
  return (unsigned)__builtin_amdgcn_mov_dpp((int)x, CTRL, 0xf, 0xf, true);
}
__device__ __forceinline__ float xrow16_max(float x) {
  auto s = __builtin_amdgcn_permlane16_swap(__float_as_uint(x), __float_as_uint(x), false, false);
  x = fmaxf(__uint_as_float(s[0]), __uint_as_float(s[1]));
  auto t = __builtin_amdgcn_permlane32_swap(__float_as_uint(x), __float_as_uint(x), false, false);
  return fmaxf(__uint_as_float(t[0]), __uint_as_float(t[1]));
}
__device__ __forceinline__ float xrow16_sum(float x) {
  auto s = __builtin_amdgcn_permlane16_swap(__float_as_uint(x), __float_as_uint(x), false, false);
  x = __uint_as_float(s[0]) + __uint_as_float(s[1]);
  auto t = __builtin_amdgcn_permlane32_swap(__float_as_uint(x), __float_as_uint(x), false, false);
  return __uint_as_float(t[0]) + __uint_as_float(t[1]);
}
__device__ __forceinline__ unsigned cvtpk(float lo, float hi) {
  typedef __bf16 bf16x2_t __attribute__((ext_vector_type(2)));
  f32x2 v = {lo, hi};
  return __builtin_bit_cast(unsigned, __builtin_convertvector(v, bf16x2_t));
}
__device__ __forceinline__ const unsigned char* mkptr(unsigned lo, unsigned hi) { return (const unsigned char*)(((unsigned long long)hi << 32) | lo); }
__device__ __forceinline__ __amdgpu_buffer_rsrc_t srd(const void* p, unsigned bytes) { return __builtin_amdgcn_make_buffer_rsrc((void*)p, 0, bytes, 0x00020000); }

__global__ void __launch_bounds__(NWAVE * 64, 2)
attn_decode(const void* a_q, const void* a_k, const void* a_v, const void* a_bt, const void* a_lens, void* a_ws, void* a_out, void* a_pad) {
  __shared__ __attribute__((aligned(16))) float lds[NWAVE * 512 + 2 * NWAVE * 4];
  u32x16 ka;
  asm volatile("s_load_dwordx16 %0, %1, 0x0" : "=s"(ka) : "s"(__builtin_amdgcn_kernarg_segment_ptr()));
  __builtin_amdgcn_s_setprio(3);
  float* lds_acc = lds;
  float* lds_ml = lds + NWAVE * 512;
  const int hp = blockIdx.x & 3, idx = blockIdx.x >> 2;
  const int tid = threadIdx.x, lane = tid & 63;
  const int wave = __builtin_amdgcn_readfirstlane(tid >> 6);
  const int h = 2 * hp + (wave & 1);
  const int slot = (wave >> 1) * NSPLIT + idx;
  const int kg = lane >> 4, t4 = (lane >> 2) & 3, c4 = lane & 3, c16 = lane & 15;
  const unsigned koffA = h * (D * 2) + t4 * 4 * ROWB + kg * 64 + c4 * 16, koffB = koffA + 2 * ROWB;
  const unsigned qoff = (h * G + t4) * (D * 2) + kg * 64 + c4 * 16;
  const unsigned slot4 = slot * 4;
  const unsigned goff = WS_GR + hp * NSPLIT * 4096 + (idx >> 3) * 512 + (idx & 7) * 64 + c4 * 16;
  const unsigned preoff = (goff + (((lane >> 2) & 7) * 8 + wave) * 4096) | ((unsigned)(lane & 32) << 26);
  const unsigned wj = (blockIdx.x >> 3) * NWAVE + wave;
  const unsigned woff = ((wj & 31) << 21) + (wj >> 6) * 4096 + (blockIdx.x & 7) * 128;
  asm volatile("s_waitcnt lgkmcnt(0)" : "+s"(ka) : "s"(slot4), "v"(koffA), "v"(koffB), "v"(qoff), "s"(woff), "v"(preoff));
  const unsigned char* q = mkptr(ka[0], ka[1]);
  const unsigned char* kc = mkptr(ka[2], ka[3]);
  const unsigned char* vc = mkptr(ka[4], ka[5]);
  const unsigned char* lens = mkptr(ka[8], ka[9]);
  unsigned char* ws = (unsigned char*)mkptr(ka[10], ka[11]);
  const unsigned char* out = mkptr(ka[12], ka[13]);
  unsigned ph[PPW];
  int len;
  {
    const unsigned char* btw = mkptr(ka[6], ka[7]) + slot4;
    asm volatile(
        "s_load_dword %0, %10, 0x0\n\t"
        "s_load_dword %1, %9, 0x0\n\t"
        "s_load_dword %2, %9, 0x400\n\t"
        "s_load_dword %3, %9, 0x800\n\t"
        "s_load_dword %4, %9, 0xc00\n\t"
        "s_load_dword %5, %9, 0x1000\n\t"
        "s_load_dword %6, %9, 0x1400\n\t"
        "s_load_dword %7, %9, 0x1800\n\t"
        "s_load_dword %8, %9, 0x1c00"
        : "=&s"(len), "=&s"(ph[0]), "=&s"(ph[1]), "=&s"(ph[2]), "=&s"(ph[3]), "=&s"(ph[4]), "=&s"(ph[5]), "=&s"(ph[6]), "=&s"(ph[7])
        : "s"(btw), "s"(lens));
  }
  __builtin_amdgcn_sched_barrier(0);
  const unsigned warm = __builtin_amdgcn_raw_buffer_load_b32(srd((wj & 32) ? vc : kc, NBPS * PAGEB), 0, woff, NT);
  const u32x4 qf = __builtin_amdgcn_raw_buffer_load_b128(srd(q, HQ * D * 2), qoff, 0, 0);
  const __amdgpu_buffer_rsrc_t wr = srd(ws, WS_END);
  const unsigned ea = __builtin_amdgcn_raw_buffer_load_b32(wr, 0, WS_EA + hp * 128, 0);
  const unsigned eb = __builtin_amdgcn_raw_buffer_load_b32(wr, 0, WS_EB + hp * 128, 0);
  const unsigned pre = __builtin_amdgcn_raw_buffer_load_b32(wr, preoff, 0, SC1);
  const unsigned pre2 = __builtin_amdgcn_raw_buffer_load_b32(srd(out, HQ * D * 2), 0, (hp * NSPLIT + idx) * 32, SC1);
  __builtin_amdgcn_sched_barrier(0);
  asm volatile("s_waitcnt lgkmcnt(0)" : "+s"(len), "+s"(ph[0]), "+s"(ph[1]), "+s"(ph[2]), "+s"(ph[3]), "+s"(ph[4]), "+s"(ph[5]), "+s"(ph[6]), "+s"(ph[7]));
  const int lenl = ((unsigned)(len - 1) < (unsigned)(NBPS * PAGE)) ? len : NBPS * PAGE;
  const unsigned kfull = len <= 0 ? 0u : (unsigned)PAGEB;
  const int nfull = lenl >> 12;
  unsigned pr[4];
  u32x4 K[WIN][4];
  auto load_k = [&](auto basec) __attribute__((always_inline)) {
    constexpr int PB = decltype(basec)::value;
#pragma unroll
    for (int i = 0; i < WIN - 1; ++i) {
      const unsigned off = ph[PB + i] * PAGEB;
      const __amdgpu_buffer_rsrc_t r = srd(kc, (PB + i < nfull) ? off + kfull : 0u);
      K[i][0] = __builtin_amdgcn_raw_buffer_load_b128(r, koffA, off, 2);
      K[i][1] = __builtin_amdgcn_raw_buffer_load_b128(r, koffA + ROWB, off, 2);
      K[i][2] = __builtin_amdgcn_raw_buffer_load_b128(r, koffB, off, 2);
      K[i][3] = __builtin_amdgcn_raw_buffer_load_b128(r, koffB + ROWB, off, 2);
      __builtin_amdgcn_sched_barrier(0);
      if (PB == 0 && i == 0) {
        const unsigned char* btr = mkptr(ka[6], ka[7]) + (min(nfull, PPW - 1) * SLOTS + (slot >> 2)) * 4;
        asm volatile("s_load_dword %0, %4, 0x0\n\ts_load_dword %1, %4, 0x100\n\ts_load_dword %2, %4, 0x200\n\ts_load_dword %3, %4, 0x300"
                     : "=&s"(pr[0]), "=&s"(pr[1]), "=&s"(pr[2]), "=&s"(pr[3]) : "s"(btr));
        __builtin_amdgcn_sched_barrier(0);
      }
    }
  };
  __builtin_amdgcn_sched_barrier(0);
  load_k(std::integral_constant<int, 0>{});
  asm volatile("s_waitcnt lgkmcnt(0)" : "+s"(pr[0]), "+s"(pr[1]), "+s"(pr[2]), "+s"(pr[3]));
  const int remq = (lenl & 4095) - (slot >> 2) * PAGE;
  const unsigned voffA = h * (D * 2) + kg * 4 * ROWB + c16 * 16, voffB = voffA + 2 * ROWB;

  f32x4 accE = {0.f, 0.f, 0.f, 0.f}, accO = {0.f, 0.f, 0.f, 0.f};
  float m = -INFINITY, l = 0.f;
  float mw0, mw1, mw2, mw3, lse;
  unsigned tag, poff, gbase, srcoff, ooff;

  auto window = [&](auto lastc, auto basec) __attribute__((always_inline)) {
    constexpr bool LAST = decltype(lastc)::value;
    constexpr int PB = decltype(basec)::value;
    constexpr int NS = LAST ? WIN : WIN - 1;
    u32x4 V[WIN][4];
    if (PB != 0) load_k(basec);
    if (LAST) {
#pragma unroll
      for (int t = 0; t < 4; ++t) {
        const unsigned off = pr[t] * PAGEB;
        K[WIN - 1][t] = __builtin_amdgcn_raw_buffer_load_b128(srd(kc, off + (unsigned)max(remq - t * 64 * PAGE, 0) * ROWB), koffA, off + (slot & 3) * ROWB, 2);
      }
      __builtin_amdgcn_sched_barrier(0);
    }
    __builtin_amdgcn_s_setprio(0);
    __builtin_amdgcn_sched_barrier(0);
    float S[WIN][4];
    float mx = -INFINITY;
#pragma unroll
    for (int i = 0; i < NS; ++i) {
      const float bias = (PB + i < nfull) ? 0.f : -INFINITY;
#pragma unroll
      for (int t = 0; t < 4; ++t) {
        f32x4 d = __builtin_amdgcn_mfma_f32_16x16x32_bf16(__builtin_bit_cast(bf16x8, K[i][t]), __builtin_bit_cast(bf16x8, qf), f32x4{0.f, 0.f, 0.f, 0.f}, 0, 0, 0);
        float x = dpp<0x00>(d[0]);
        x += dpp<0x55>(d[1]);
        x += dpp<0xAA>(d[2]);
        x += dpp<0xFF>(d[3]);
        if (i < WIN - 1) x = fmaf(x, C2, bias);
        else x = (4 * kg + (slot & 3) < remq - t * 64 * PAGE) ? x * C2 : -INFINITY;
        S[i][t] = x;
        mx = fmaxf(mx, x);
      }
    }
    mx = xrow16_max(mx);
    const float mn = fmaxf(m, mx);
    const float msub = (mn == -INFINITY) ? 0.f : mn;
    {
      const float alpha = __builtin_amdgcn_exp2f(m - msub);
      l *= alpha; accE *= alpha; accO *= alpha;
    }
    m = mn;
    u32x2 P[WIN][4];
#pragma unroll
    for (int i = 0; i < NS; ++i) {
      float p[4];
#pragma unroll
      for (int t = 0; t < 4; ++t) { p[t] = __builtin_amdgcn_exp2f(S[i][t] - msub); l += p[t]; }
      const unsigned p01 = cvtpk(p[0], p[1]), p23 = cvtpk(p[2], p[3]);
#pragma unroll
      for (int jj = 0; jj < 4; ++jj) P[i][jj] = u32x2{c4 == jj ? p01 : 0u, c4 == jj ? p23 : 0u};
    }
    if (LAST) {
      const float lt = xrow16_sum(l);
      if (lane < 16 && c4 == 0) *reinterpret_cast<float2*>(lds_ml + (((wave & 1) * 4 + t4) * 4 + (wave >> 1)) * 2) = make_float2(m, lt);
    }
    __builtin_amdgcn_sched_barrier(0);
    if (LAST) {
#pragma unroll
      for (int t = 0; t < 4; ++t) {
        const unsigned off = pr[t] * PAGEB;
        V[WIN - 1][t] = __builtin_amdgcn_raw_buffer_load_b128(srd(vc, off + (unsigned)max(remq - t * 64 * PAGE, 0) * ROWB), voffA, off + (slot & 3) * ROWB, 0);
      }
    }
#pragma unroll
    for (int i = 0; i < WIN - 1; ++i) {
      const unsigned off = ph[PB + i] * PAGEB;
      const __amdgpu_buffer_rsrc_t r = srd(vc, (PB + i < nfull) ? off + PAGEB : 0u);
      V[i][0] = __builtin_amdgcn_raw_buffer_load_b128(r, voffA, off, 0);
      V[i][1] = __builtin_amdgcn_raw_buffer_load_b128(r, voffA + ROWB, off, 0);
      V[i][2] = __builtin_amdgcn_raw_buffer_load_b128(r, voffB, off, 0);
      V[i][3] = __builtin_amdgcn_raw_buffer_load_b128(r, voffB + ROWB, off, 0);
    }
    __builtin_amdgcn_sched_barrier(0);
    if (LAST) {
      __syncthreads();
      const f32x4 a = *reinterpret_cast<const f32x4*>(lds_ml + wave * 8);
      const f32x4 b = *reinterpret_cast<const f32x4*>(lds_ml + wave * 8 + 4);
      const float M = fmaxf(fmaxf(a[0], a[2]), fmaxf(b[0], b[2]));
      const float ms = (M == -INFINITY) ? 0.f : M;
      const float w0 = __builtin_amdgcn_exp2f(a[0] - ms), w1 = __builtin_amdgcn_exp2f(a[2] - ms), w2 = __builtin_amdgcn_exp2f(b[0] - ms), w3 = __builtin_amdgcn_exp2f(b[2] - ms);
      const float L = (w0 * a[1] + w1 * a[3]) + (w2 * b[1] + w3 * b[3]);
      const float inv = (L > 0.f) ? __builtin_amdgcn_rcpf(L) : 0.f;
      mw0 = w0 * inv; mw1 = w1 * inv; mw2 = w2 * inv; mw3 = w3 * inv;
      lse = M + __builtin_amdgcn_logf(L);
      tag = ea * TC1 + eb * TC2 + TC3;
      poff = (WS_GR + ((hp * NSPLIT + idx) * 8 + wave) * 512 + lane * 8) | ((unsigned)lane << 31);
      gbase = goff + (lane >> 2) * 4 * 4096;
      srcoff = (wave >> 2) * 512 + (kg * 16 + (wave & 3) * 4 + c4) * 4 + t4;
      ooff = (((hp * 8 + (idx >> 3)) * D + (idx & 7) * 16 + c4 * 4 + (kg & 1) * 2 + (kg >> 1)) * 2) | ((unsigned)t4 << 30);
      asm volatile("" : "+v"(mw0), "+v"(mw1), "+v"(mw2), "+v"(mw3), "+v"(lse), "+v"(tag), "+v"(poff), "+v"(gbase), "+v"(srcoff), "+v"(ooff) : "v"(warm), "v"(pre), "v"(pre2));
    }
    __builtin_amdgcn_sched_barrier(0);
#pragma unroll
    for (int ii = 0; ii < NS; ++ii) {
      const int i = LAST ? (ii + WIN - 1) % WIN : ii;
      unsigned lo01[4], hi01[4];
#pragma unroll
      for (int wd = 0; wd < 4; ++wd) { lo01[wd] = __builtin_amdgcn_perm(V[i][1][wd], V[i][0][wd], 0x05040100u); hi01[wd] = __builtin_amdgcn_perm(V[i][1][wd], V[i][0][wd], 0x07060302u); }
      __builtin_amdgcn_sched_barrier(0);
#pragma unroll
      for (int wd = 0; wd < 4; ++wd) {
        const u32x2 lo = {lo01[wd], __builtin_amdgcn_perm(V[i][3][wd], V[i][2][wd], 0x05040100u)};
        const u32x2 hi = {hi01[wd], __builtin_amdgcn_perm(V[i][3][wd], V[i][2][wd], 0x07060302u)};
        accE = __builtin_amdgcn_mfma_f32_16x16x16bf16_1k(__builtin_bit_cast(s16x4, lo), __builtin_bit_cast(s16x4, P[i][wd]), accE, 0, 0, 0);
        accO = __builtin_amdgcn_mfma_f32_16x16x16bf16_1k(__builtin_bit_cast(s16x4, hi), __builtin_bit_cast(s16x4, P[i][wd]), accO, 0, 0, 0);
        __builtin_amdgcn_sched_barrier(0);
      }
    }
  };

  auto finish = [&]() __attribute__((always_inline)) {
    *reinterpret_cast<f32x4*>(lds_acc + wave * 512 + lane * 4) = accE;
    *reinterpret_cast<f32x4*>(lds_acc + wave * 512 + 256 + lane * 4) = accO;
    const float* src = lds_acc + srcoff;
    __syncthreads();
    {
      f32x2 x[4];
#pragma unroll
      for (int k = 0; k < 4; ++k) x[k] = f32x2{src[k * 1024], src[k * 1024 + 256]};
      f32x2 o = x[0] * mw0;
      o = __builtin_elementwise_fma(x[1], f32x2{mw1, mw1}, o);
      o = __builtin_elementwise_fma(x[2], f32x2{mw2, mw2}, o);
      o = __builtin_elementwise_fma(x[3], f32x2{mw3, mw3}, o);
      const unsigned pk = cvtpk(o[0], o[1]);
      const unsigned pk2 = dppu<0xB1>(pk);
      __builtin_amdgcn_raw_buffer_store_b128(u32x4{(unsigned)__builtin_amdgcn_bitop3_b32(tag, pk, pk2, 0x96), __float_as_uint(lse), pk, pk2}, wr, poff, 0, SC1);
    }
    if (wave == 0) {
      const __amdgpu_buffer_rsrc_t orr = srd(out, HQ * D * 2);
      unsigned long long p0, p1, p2, p3;
      unsigned cnt;
#define QP " quad_perm:[1,0,3,2] row_mask:0xf bank_mask:0xf bound_ctrl:1\n\t"
#define RR4 " row_ror:4 row_mask:0xf bank_mask:0xf bound_ctrl:1\n\t"
#define RR8 " row_ror:8 row_mask:0xf bank_mask:0xf bound_ctrl:1\n\t"
      asm volatile(
          "s_mov_b64 %0, -1\n\ts_mov_b64 %1, 0\n\ts_mov_b64 %2, 0\n\ts_mov_b64 %3, 0\n\t"
          "v_mbcnt_lo_u32_b32 v205, -1, 0\n\tv_mbcnt_hi_u32_b32 v205, -1, v205\n\tv_mov_b32 v209, v205\n\tv_mov_b32 v213, v205\n\t"
          "s_sleep 8\n\t"
          "s_mov_b32 %4, 0x200000\n"
          ".Lpoll%=:\n\t"
          "s_mov_b64 exec, %0\n\tbuffer_load_dwordx4 v[200:203], %6, %7, 0 offen sc1\n\t"
          "s_mov_b64 exec, %1\n\tbuffer_load_dwordx4 v[204:207], %6, %7, %10 offen sc1\n\t"
          "s_mov_b64 exec, %2\n\tbuffer_load_dwordx4 v[208:211], %6, %7, %11 offen sc1\n\t"
          "s_mov_b64 exec, %3\n\tbuffer_load_dwordx4 v[212:215], %6, %7, %12 offen sc1\n\t"
          "s_mov_b64 exec, -1\n\t"
          "s_waitcnt vmcnt(0)\n\t"
          "v_max3_f32 v221, v201, v205, %13\n\t"
          "v_max3_f32 v221, v221, v209, v213\n\t"
          "v_xor_b32_dpp v216, v201, v201" QP
          "v_xor_b32_dpp v217, v205, v205" QP
          "v_max_f32_dpp v221, v221, v221" RR4
          "v_xor_b32_dpp v218, v209, v209" QP
          "v_xor_b32_dpp v219, v213, v213" QP
          "v_max_f32_dpp v221, v221, v221" RR8
          "v_bitop3_b32 v224, v200, v202, v203 bitop3:0x96\n\t"
          "v_mov_b32 v222, v221\n\t"
          "v_bitop3_b32 v225, v204, v206, v207 bitop3:0x96\n\t"
          "v_bitop3_b32 v226, v208, v210, v211 bitop3:0x96\n\t"
          "v_permlane16_swap_b32 v221, v222\n\t"
          "v_bitop3_b32 v227, v212, v214, v215 bitop3:0x96\n\t"
          "v_max_f32 v221, v221, v222\n\t"
          "v_bitop3_b32 v216, v224, v216, %5 bitop3:0xde\n\t"
          "v_mov_b32 v222, v221\n\t"
          "v_bitop3_b32 v217, v225, v217, %5 bitop3:0xde\n\t"
          "v_bitop3_b32 v218, v226, v218, %5 bitop3:0xde\n\t"
          "v_permlane32_swap_b32 v221, v222\n\t"
          "v_bitop3_b32 v219, v227, v219, %5 bitop3:0xde\n\t"
          "v_or3_b32 v220, v216, v217, v218\n\t"
          "v_or_b32 v220, v220, v219\n\t"
          "v_cmp_ne_u32 vcc, 0, v220\n\t"
          "s_cbranch_vccz .Ldone%=\n\t"
          "v_or_b32_dpp v216, v216, v216" QP
          "v_or_b32_dpp v217, v217, v217" QP
          "v_or_b32_dpp v218, v218, v218" QP
          "v_or_b32_dpp v219, v219, v219" QP
          "v_cmp_ne_u32 %0, 0, v216\n\t"
          "v_cmp_ne_u32 %1, 0, v217\n\t"
          "v_cmp_ne_u32 %2, 0, v218\n\t"
          "v_cmp_ne_u32 %3, 0, v219\n\t"
          "s_bcnt1_i32_b64 vcc_lo, %0\n\t"
          "s_cmp_gt_u32 vcc_lo, 32\n\t"
          "s_cselect_b64 %1, 0, %1\n\ts_cselect_b64 %2, 0, %2\n\ts_cselect_b64 %3, 0, %3\n\t"
          "s_cbranch_scc0 .Lns%=\n\t"
          "s_sleep 6\n"
          ".Lns%=:\n\t"
          "s_sub_u32 %4, %4, 1\n\t"
          "s_cbranch_scc0 .Lpoll%=\n"
          ".Ldone%=:\n\t"
          "v_max_f32 v221, v221, v222\n\t"
          "v_lshlrev_b32 v224, 16, v202\n\t"
          "v_sub_f32 v201, v201, v221\n\t"
          "v_sub_f32 v205, v205, v221\n\t"
          "v_sub_f32 v209, v209, v221\n\t"
          "v_sub_f32 v213, v213, v221\n\t"
          "v_exp_f32 v200, v201\n\t"
          "v_exp_f32 v204, v205\n\t"
          "v_exp_f32 v208, v209\n\t"
          "v_exp_f32 v212, v213\n\t"
          "v_and_b32 v225, %14, v202\n\t"
          "v_lshlrev_b32 v226, 16, v203\n\t"
          "v_and_b32 v227, %14, v203\n\t"
          "v_lshlrev_b32 v228, 16, v206\n\t"
          "v_and_b32 v229, %14, v206\n\t"
          "v_pk_mul_f32 v[224:225], v[224:225], v[200:201] op_sel_hi:[1,0]\n\t"
          "v_pk_mul_f32 v[226:227], v[226:227], v[200:201] op_sel_hi:[1,0]\n\t"
          "v_lshlrev_b32 v230, 16, v207\n\t"
          "v_and_b32 v231, %14, v207\n\t"
          "v_add_f32 v223, v200, v204\n\t"
          "v_pk_fma_f32 v[224:225], v[228:229], v[204:205], v[224:225] op_sel_hi:[1,0,1]\n\t"
          "v_pk_fma_f32 v[226:227], v[230:231], v[204:205], v[226:227] op_sel_hi:[1,0,1]\n\t"
          "v_lshlrev_b32 v228, 16, v210\n\t"
          "v_and_b32 v229, %14, v210\n\t"
          "v_lshlrev_b32 v230, 16, v211\n\t"
          "v_and_b32 v231, %14, v211\n\t"
          "v_add_f32 v222, v208, v212\n\t"
          "v_pk_fma_f32 v[224:225], v[228:229], v[208:209], v[224:225] op_sel_hi:[1,0,1]\n\t"
          "v_pk_fma_f32 v[226:227], v[230:231], v[208:209], v[226:227] op_sel_hi:[1,0,1]\n\t"
          "v_add_f32 v223, v223, v222\n\t"
          "v_lshlrev_b32 v228, 16, v214\n\t"
          "v_and_b32 v229, %14, v214\n\t"
          "v_add_f32_dpp v223, v223, v223" RR4
          "v_lshlrev_b32 v230, 16, v215\n\t"
          "v_and_b32 v231, %14, v215\n\t"
          "v_add_f32_dpp v223, v223, v223" RR8
          "v_pk_fma_f32 v[224:225], v[228:229], v[212:213], v[224:225] op_sel_hi:[1,0,1]\n\t"
          "v_pk_fma_f32 v[226:227], v[230:231], v[212:213], v[226:227] op_sel_hi:[1,0,1]\n\t"
          "v_mov_b32 v222, v223\n\t"
          "s_nop 0\n\t"
          "v_permlane32_swap_b32 v224, v225\n\t"
          "v_permlane32_swap_b32 v226, v227\n\t"
          "v_permlane16_swap_b32 v223, v222\n\t"
          "v_add_f32 v224, v224, v225\n\t"
          "v_add_f32 v226, v226, v227\n\t"
          "v_add_f32 v223, v223, v222\n\t"
          "s_nop 0\n\t"
          "v_mov_b32 v222, v223\n\t"
          "v_permlane16_swap_b32 v224, v226\n\t"
          "s_nop 0\n\t"
          "v_permlane32_swap_b32 v223, v222\n\t"
          "v_add_f32 v224, v224, v226\n\t"
          "v_add_f32 v223, v223, v222\n\t"
          "v_rcp_f32 v222, v223\n\t"
          "v_add_f32_dpp v224, v224, v224" RR4
          "v_cmp_lt_f32 vcc, 0, v223\n\t"
          "s_nop 0\n\t"
          "v_add_f32_dpp v224, v224, v224" RR8
          "v_cndmask_b32 v222, 0, v222, vcc\n\t"
          "v_mul_f32 v224, v224, v222\n\t"
          "v_cvt_pk_bf16_f32 v224, v224, v224\n\t"
          "buffer_store_short v224, %8, %9, 0 offen sc1"
          : "=&s"(p0), "=&s"(p1), "=&s"(p2), "=&s"(p3), "=&s"(cnt)
          : "v"(tag), "v"(gbase), "s"(wr), "v"(ooff), "s"(orr), "s"(3 * 4096), "s"(1 * 4096), "s"(2 * 4096), "s"(-3.0e38f), "s"(0xffff0000u)
          : "memory", "vcc", "scc", "v200", "v201", "v202", "v203", "v204", "v205", "v206", "v207", "v208", "v209", "v210", "v211", "v212", "v213", "v214", "v215",
            "v216", "v217", "v218", "v219", "v220", "v221", "v222", "v223", "v224", "v225", "v226", "v227", "v228", "v229", "v230", "v231");
      if (idx == 0 && tid == 0) {
        __hip_atomic_store((gu32*)(ws + WS_EA + hp * 128), ea + 1u, __ATOMIC_RELAXED, __HIP_MEMORY_SCOPE_AGENT);
        __hip_atomic_store((gu32*)(ws + WS_EB + hp * 128), eb + 1u, __ATOMIC_RELAXED, __HIP_MEMORY_SCOPE_AGENT);
      }
    }
  };

  if (__builtin_expect(nfull <= WIN - 1, 1)) {
    window(std::true_type{}, std::integral_constant<int, 0>{});
    finish();
  } else {
    window(std::false_type{}, std::integral_constant<int, 0>{});
    __builtin_amdgcn_s_setprio(3);
    window(std::true_type{}, std::integral_constant<int, WIN - 1>{});
    finish();
  }
}

extern "C" void kernel_launch(void* const* d_in, const int* in_sizes, int n_in, void* d_out, int out_size, void* d_ws, size_t ws_size, hipStream_t stream) {
  attn_decode<<<dim3(NWG), dim3(NWAVE * 64), 0, stream>>>(d_in[0], d_in[1], d_in[2], d_in[3], d_in[4], d_ws, d_out, d_ws);
}
```

```cpp
#include <hip/hip_runtime.h>
#include <hip/hip_bf16.h>
#include <stdint.h>
#include <type_traits>

typedef unsigned u32x16 __attribute__((ext_vector_type(16)));
typedef unsigned u32x4 __attribute__((ext_vector_type(4)));
typedef unsigned u32x2 __attribute__((ext_vector_type(2)));
typedef float f32x4 __attribute__((ext_vector_type(4)));
typedef float f32x2 __attribute__((ext_vector_type(2)));
typedef short s16x4 __attribute__((ext_vector_type(4)));
typedef __bf16 bf16x8 __attribute__((ext_vector_type(8)));
typedef __attribute__((address_space(1))) unsigned gu32;

constexpr int NT = 2, SC1 = 16;
constexpr int HQ = 32, HKV = 8, G = 4, D = 128, PAGE = 16, NBPS = 2048;
constexpr int NWAVE = 8, NWG = 256, NGRP = 4, NSPLIT = NWG / NGRP;
constexpr int SLOTS = 256;
constexpr int WIN = 5;
constexpr int PPW = NBPS / SLOTS;
constexpr int ROWB = HKV * D * 2;
constexpr int PAGEB = PAGE * ROWB;
constexpr float C2 = 0.088388347648318440f * 1.4426950408889634f;
static_assert(PPW == 8 && 2 * (WIN - 1) == PPW, "the prologue loads 8 block-table entries; two windows cover them");
constexpr unsigned WS_EA = 0, WS_GR = 512, GR_BYTES = NGRP * NSPLIT * 8 * 512, WS_EB = WS_GR + GR_BYTES, WS_END = WS_EB + 512;
constexpr unsigned TC1 = 0x9E3779B1u, TC2 = 0x85EBCA77u, TC3 = 0x27D4EB2Fu;
constexpr bool tag_ok() {
  for (unsigned b = 0; b < 256; ++b) { const unsigned p = b * 0x01010101u; if (p * TC1 + p * TC2 + TC3 == p) return false; }
  return TC1 + TC2 != 0u;
}
static_assert(tag_ok(), "the tag must differ from any constant-byte fill and change with either epoch word");

template <int CTRL> __device__ __forceinline__ float dpp(float x) {
  return __builtin_bit_cast(float, __builtin_amdgcn_mov_dpp(__builtin_bit_cast(int, x), CTRL, 0xf, 0xf, true));
}
template <int CTRL> __device__ __forceinline__ unsigned dppu(unsigned x) {
  return (unsigned)__builtin_amdgcn_mov_dpp((int)x, CTRL, 0xf, 0xf, true);
}
__device__ __forceinline__ float xrow16_max(float x) {
  auto s = __builtin_amdgcn_permlane16_swap(__float_as_uint(x), __float_as_uint(x), false, false);
  x = fmaxf(__uint_as_float(s[0]), __uint_as_float(s[1]));
  auto t = __builtin_amdgcn_permlane32_swap(__float_as_uint(x), __float_as_uint(x), false, false);
  return fmaxf(__uint_as_float(t[0]), __uint_as_float(t[1]));
}
__device__ __forceinline__ float xrow16_sum(float x) {
  auto s = __builtin_amdgcn_permlane16_swap(__float_as_uint(x), __float_as_uint(x), false, false);
  x = __uint_as_float(s[0]) + __uint_as_float(s[1]);
  auto t = __builtin_amdgcn_permlane32_swap(__float_as_uint(x), __float_as_uint(x), false, false);
  return __uint_as_float(t[0]) + __uint_as_float(t[1]);
}
__device__ __forceinline__ unsigned cvtpk(float lo, float hi) {
  typedef __bf16 bf16x2_t __attribute__((ext_vector_type(2)));
  f32x2 v = {lo, hi};
  return __builtin_bit_cast(unsigned, __builtin_convertvector(v, bf16x2_t));
}
__device__ __forceinline__ const unsigned char* mkptr(unsigned lo, unsigned hi) { return (const unsigned char*)(((unsigned long long)hi << 32) | lo); }
__device__ __forceinline__ __amdgpu_buffer_rsrc_t srd(const void* p, unsigned bytes) { return __builtin_amdgcn_make_buffer_rsrc((void*)p, 0, bytes, 0x00020000); }

__global__ void __launch_bounds__(NWAVE * 64, 2)
attn_decode(const void* a_q, const void* a_k, const void* a_v, const void* a_bt, const void* a_lens, void* a_ws, void* a_out, void* a_pad) {
  __shared__ __attribute__((aligned(16))) float lds[NWAVE * 512 + 2 * NWAVE * 4];
  u32x16 ka;
  asm volatile("s_load_dwordx16 %0, %1, 0x0" : "=s"(ka) : "s"(__builtin_amdgcn_kernarg_segment_ptr()));
  __builtin_amdgcn_s_setprio(3);
  float* lds_acc = lds;
  float* lds_ml = lds + NWAVE * 512;
  const int hp = blockIdx.x & 3, idx = blockIdx.x >> 2;
  const int tid = threadIdx.x, lane = tid & 63;
  const int wave = __builtin_amdgcn_readfirstlane(tid >> 6);
  const int h = 2 * hp + (wave & 1);
  const int slot = (wave >> 1) * NSPLIT + idx;
  const int kg = lane >> 4, t4 = (lane >> 2) & 3, c4 = lane & 3, c16 = lane & 15;
  const unsigned koffA = h * (D * 2) + t4 * 4 * ROWB + kg * 64 + c4 * 16, koffB = koffA + 2 * ROWB;
  const unsigned qoff = (h * G + t4) * (D * 2) + kg * 64 + c4 * 16;
  const unsigned slot4 = slot * 4;
  const unsigned goff = WS_GR + hp * NSPLIT * 4096 + (idx >> 3) * 512 + (idx & 7) * 64 + c4 * 16;
  const unsigned preoff = (goff + (((lane >> 2) & 7) * 8 + wave) * 4096) | ((unsigned)(lane & 32) << 26);
  const unsigned wj = (blockIdx.x >> 3) * NWAVE + wave;
  const unsigned woff = ((wj & 31) << 21) + (wj >> 6) * 4096 + (blockIdx.x & 7) * 128;
  asm volatile("s_waitcnt lgkmcnt(0)" : "+s"(ka) : "s"(slot4), "v"(koffA), "v"(koffB), "v"(qoff), "s"(woff), "v"(preoff));
  const unsigned char* q = mkptr(ka[0], ka[1]);
  const unsigned char* kc = mkptr(ka[2], ka[3]);
  const unsigned char* vc = mkptr(ka[4], ka[5]);
  const unsigned char* lens = mkptr(ka[8], ka[9]);
  unsigned char* ws = (unsigned char*)mkptr(ka[10], ka[11]);
  const unsigned char* out = mkptr(ka[12], ka[13]);
  unsigned ph[PPW];
  int len;
  {
    const unsigned char* btw = mkptr(ka[6], ka[7]) + slot4;
    asm volatile(
        "s_load_dword %0, %10, 0x0\n\t"
        "s_load_dword %1, %9, 0x0\n\t"
        "s_load_dword %2, %9, 0x400\n\t"
        "s_load_dword %3, %9, 0x800\n\t"
        "s_load_dword %4, %9, 0xc00\n\t"
        "s_load_dword %5, %9, 0x1000\n\t"
        "s_load_dword %6, %9, 0x1400\n\t"
        "s_load_dword %7, %9, 0x1800\n\t"
        "s_load_dword %8, %9, 0x1c00"
        : "=&s"(len), "=&s"(ph[0]), "=&s"(ph[1]), "=&s"(ph[2]), "=&s"(ph[3]), "=&s"(ph[4]), "=&s"(ph[5]), "=&s"(ph[6]), "=&s"(ph[7])
        : "s"(btw), "s"(lens));
  }
  __builtin_amdgcn_sched_barrier(0);
  const unsigned warm = __builtin_amdgcn_raw_buffer_load_b32(srd((wj & 32) ? vc : kc, NBPS * PAGEB), 0, woff, NT);
  const u32x4 qf = __builtin_amdgcn_raw_buffer_load_b128(srd(q, HQ * D * 2), qoff, 0, 0);
  const __amdgpu_buffer_rsrc_t wr = srd(ws, WS_END);
  const unsigned ea = __builtin_amdgcn_raw_buffer_load_b32(wr, 0, WS_EA + hp * 128, 0);
  const unsigned eb = __builtin_amdgcn_raw_buffer_load_b32(wr, 0, WS_EB + hp * 128, 0);
  const unsigned pre = __builtin_amdgcn_raw_buffer_load_b32(wr, preoff, 0, SC1);
  const unsigned pre2 = __builtin_amdgcn_raw_buffer_load_b32(srd(out, HQ * D * 2), 0, (hp * NSPLIT + idx) * 32, SC1);
  __builtin_amdgcn_sched_barrier(0);
  asm volatile("s_waitcnt lgkmcnt(0)" : "+s"(len), "+s"(ph[0]), "+s"(ph[1]), "+s"(ph[2]), "+s"(ph[3]), "+s"(ph[4]), "+s"(ph[5]), "+s"(ph[6]), "+s"(ph[7]));
  const int lenl = ((unsigned)(len - 1) < (unsigned)(NBPS * PAGE)) ? len : NBPS * PAGE;
  const unsigned kfull = len <= 0 ? 0u : (unsigned)PAGEB;
  const int nfull = lenl >> 12;
  unsigned pr[4];
  u32x4 K[WIN][4];
  auto load_k = [&](auto basec) __attribute__((always_inline)) {
    constexpr int PB = decltype(basec)::value;
#pragma unroll
    for (int i = 0; i < WIN - 1; ++i) {
      const unsigned off = ph[PB + i] * PAGEB;
      const __amdgpu_buffer_rsrc_t r = srd(kc, (PB + i < nfull) ? off + kfull : 0u);
      K[i][0] = __builtin_amdgcn_raw_buffer_load_b128(r, koffA, off, NT);
      K[i][1] = __builtin_amdgcn_raw_buffer_load_b128(r, koffA + ROWB, off, NT);
      K[i][2] = __builtin_amdgcn_raw_buffer_load_b128(r, koffB, off, NT);
      K[i][3] = __builtin_amdgcn_raw_buffer_load_b128(r, koffB + ROWB, off, NT);
      __builtin_amdgcn_sched_barrier(0);
      if (PB == 0 && i == 0) {
        const unsigned char* btr = mkptr(ka[6], ka[7]) + (min(nfull, PPW - 1) * SLOTS + (slot >> 2)) * 4;
        asm volatile("s_load_dword %0, %4, 0x0\n\ts_load_dword %1, %4, 0x100\n\ts_load_dword %2, %4, 0x200\n\ts_load_dword %3, %4, 0x300"
                     : "=&s"(pr[0]), "=&s"(pr[1]), "=&s"(pr[2]), "=&s"(pr[3]) : "s"(btr));
        __builtin_amdgcn_sched_barrier(0);
      }
    }
  };
  __builtin_amdgcn_sched_barrier(0);
  load_k(std::integral_constant<int, 0>{});
  asm volatile("s_waitcnt lgkmcnt(0)" : "+s"(pr[0]), "+s"(pr[1]), "+s"(pr[2]), "+s"(pr[3]));
  const int remq = (lenl & 4095) - (slot >> 2) * PAGE;
  const unsigned voffA = h * (D * 2) + kg * 4 * ROWB + c16 * 16, voffB = voffA + 2 * ROWB;

  f32x4 accE = {0.f, 0.f, 0.f, 0.f}, accO = {0.f, 0.f, 0.f, 0.f};
  float m = -INFINITY, l = 0.f;
  float mw0, mw1, mw2, mw3, lse;
  unsigned tag, poff, gbase, srcoff, ooff;

  auto window = [&](auto lastc, auto basec) __attribute__((always_inline)) {
    constexpr bool LAST = decltype(lastc)::value;
    constexpr int PB = decltype(basec)::value;
    constexpr int NS = LAST ? WIN : WIN - 1;
    u32x4 V[WIN][4];
    if (PB != 0) load_k(basec);
    if (LAST) {
#pragma unroll
      for (int t = 0; t < 4; ++t) {
        const unsigned off = pr[t] * PAGEB;
        K[WIN - 1][t] = __builtin_amdgcn_raw_buffer_load_b128(srd(kc, off + (unsigned)max(remq - t * 64 * PAGE, 0) * ROWB), koffA, off + (slot & 3) * ROWB, NT);
      }
      __builtin_amdgcn_sched_barrier(0);
    }
    __builtin_amdgcn_s_setprio(0);
    __builtin_amdgcn_sched_barrier(0);
    float S[WIN][4];
    float mx = -INFINITY;
#pragma unroll
    for (int i = 0; i < NS; ++i) {
      const float bias = (PB + i < nfull) ? 0.f : -INFINITY;
#pragma unroll
      for (int t = 0; t < 4; ++t) {
        f32x4 d = __builtin_amdgcn_mfma_f32_16x16x32_bf16(__builtin_bit_cast(bf16x8, K[i][t]), __builtin_bit_cast(bf16x8, qf), f32x4{0.f, 0.f, 0.f, 0.f}, 0, 0, 0);
        float x = dpp<0x00>(d[0]);
        x += dpp<0x55>(d[1]);
        x += dpp<0xAA>(d[2]);
        x += dpp<0xFF>(d[3]);
        if (i < WIN - 1) x = fmaf(x, C2, bias);
        else x = (4 * kg + (slot & 3) < remq - t * 64 * PAGE) ? x * C2 : -INFINITY;
        S[i][t] = x;
        mx = fmaxf(mx, x);
      }
    }
    mx = xrow16_max(mx);
    const float mn = fmaxf(m, mx);
    const float msub = (mn == -INFINITY) ? 0.f : mn;
    {
      const float alpha = __builtin_amdgcn_exp2f(m - msub);
      l *= alpha; accE *= alpha; accO *= alpha;
    }
    m = mn;
    u32x2 P[WIN][4];
#pragma unroll
    for (int i = 0; i < NS; ++i) {
      float p[4];
#pragma unroll
      for (int t = 0; t < 4; ++t) { p[t] = __builtin_amdgcn_exp2f(S[i][t] - msub); l += p[t]; }
      const unsigned p01 = cvtpk(p[0], p[1]), p23 = cvtpk(p[2], p[3]);
#pragma unroll
      for (int jj = 0; jj < 4; ++jj) P[i][jj] = u32x2{c4 == jj ? p01 : 0u, c4 == jj ? p23 : 0u};
    }
    if (LAST) {
      const float lt = xrow16_sum(l);
      if (lane < 16 && c4 == 0) *reinterpret_cast<float2*>(lds_ml + (((wave & 1) * 4 + t4) * 4 + (wave >> 1)) * 2) = make_float2(m, lt);
    }
    __builtin_amdgcn_sched_barrier(0);
    if (LAST) {
#pragma unroll
      for (int t = 0; t < 4; ++t) {
        const unsigned off = pr[t] * PAGEB;
        V[WIN - 1][t] = __builtin_amdgcn_raw_buffer_load_b128(srd(vc, off + (unsigned)max(remq - t * 64 * PAGE, 0) * ROWB), voffA, off + (slot & 3) * ROWB, NT);
      }
    }
#pragma unroll
    for (int i = 0; i < WIN - 1; ++i) {
      const unsigned off = ph[PB + i] * PAGEB;
      const __amdgpu_buffer_rsrc_t r = srd(vc, (PB + i < nfull) ? off + PAGEB : 0u);
      V[i][0] = __builtin_amdgcn_raw_buffer_load_b128(r, voffA, off, NT);
      V[i][1] = __builtin_amdgcn_raw_buffer_load_b128(r, voffA + ROWB, off, NT);
      V[i][2] = __builtin_amdgcn_raw_buffer_load_b128(r, voffB, off, NT);
      V[i][3] = __builtin_amdgcn_raw_buffer_load_b128(r, voffB + ROWB, off, NT);
    }
    __builtin_amdgcn_sched_barrier(0);
    if (LAST) {
      __syncthreads();
      const f32x4 a = *reinterpret_cast<const f32x4*>(lds_ml + wave * 8);
      const f32x4 b = *reinterpret_cast<const f32x4*>(lds_ml + wave * 8 + 4);
      const float M = fmaxf(fmaxf(a[0], a[2]), fmaxf(b[0], b[2]));
      const float ms = (M == -INFINITY) ? 0.f : M;
      const float w0 = __builtin_amdgcn_exp2f(a[0] - ms), w1 = __builtin_amdgcn_exp2f(a[2] - ms), w2 = __builtin_amdgcn_exp2f(b[0] - ms), w3 = __builtin_amdgcn_exp2f(b[2] - ms);
      const float L = (w0 * a[1] + w1 * a[3]) + (w2 * b[1] + w3 * b[3]);
      const float inv = (L > 0.f) ? __builtin_amdgcn_rcpf(L) : 0.f;
      mw0 = w0 * inv; mw1 = w1 * inv; mw2 = w2 * inv; mw3 = w3 * inv;
      lse = M + __builtin_amdgcn_logf(L);
      tag = ea * TC1 + eb * TC2 + TC3;
      poff = (WS_GR + ((hp * NSPLIT + idx) * 8 + wave) * 512 + lane * 8) | ((unsigned)lane << 31);
      gbase = goff + (lane >> 2) * 4 * 4096;
      srcoff = (wave >> 2) * 512 + (kg * 16 + (wave & 3) * 4 + c4) * 4 + t4;
      ooff = (((hp * 8 + (idx >> 3)) * D + (idx & 7) * 16 + c4 * 4 + (kg & 1) * 2 + (kg >> 1)) * 2) | ((unsigned)t4 << 30);
      asm volatile("" : "+v"(mw0), "+v"(mw1), "+v"(mw2), "+v"(mw3), "+v"(lse), "+v"(tag), "+v"(poff), "+v"(gbase), "+v"(srcoff), "+v"(ooff) : "v"(warm), "v"(pre), "v"(pre2));
    }
    __builtin_amdgcn_sched_barrier(0);
#pragma unroll
    for (int ii = 0; ii < NS; ++ii) {
      const int i = LAST ? (ii + WIN - 1) % WIN : ii;
      unsigned lo01[4], hi01[4];
#pragma unroll
      for (int wd = 0; wd < 4; ++wd) { lo01[wd] = __builtin_amdgcn_perm(V[i][1][wd], V[i][0][wd], 0x05040100u); hi01[wd] = __builtin_amdgcn_perm(V[i][1][wd], V[i][0][wd], 0x07060302u); }
      __builtin_amdgcn_sched_barrier(0);
#pragma unroll
      for (int wd = 0; wd < 4; ++wd) {
        const u32x2 lo = {lo01[wd], __builtin_amdgcn_perm(V[i][3][wd], V[i][2][wd], 0x05040100u)};
        const u32x2 hi = {hi01[wd], __builtin_amdgcn_perm(V[i][3][wd], V[i][2][wd], 0x07060302u)};
        accE = __builtin_amdgcn_mfma_f32_16x16x16bf16_1k(__builtin_bit_cast(s16x4, lo), __builtin_bit_cast(s16x4, P[i][wd]), accE, 0, 0, 0);
        accO = __builtin_amdgcn_mfma_f32_16x16x16bf16_1k(__builtin_bit_cast(s16x4, hi), __builtin_bit_cast(s16x4, P[i][wd]), accO, 0, 0, 0);
        __builtin_amdgcn_sched_barrier(0);
      }
    }
  };

  auto finish = [&]() __attribute__((always_inline)) {
    *reinterpret_cast<f32x4*>(lds_acc + wave * 512 + lane * 4) = accE;
    *reinterpret_cast<f32x4*>(lds_acc + wave * 512 + 256 + lane * 4) = accO;
    const float* src = lds_acc + srcoff;
    __syncthreads();
    {
      f32x2 x[4];
#pragma unroll
      for (int k = 0; k < 4; ++k) x[k] = f32x2{src[k * 1024], src[k * 1024 + 256]};
      f32x2 o = x[0] * mw0;
      o = __builtin_elementwise_fma(x[1], f32x2{mw1, mw1}, o);
      o = __builtin_elementwise_fma(x[2], f32x2{mw2, mw2}, o);
      o = __builtin_elementwise_fma(x[3], f32x2{mw3, mw3}, o);
      const unsigned pk = cvtpk(o[0], o[1]);
      const unsigned pk2 = dppu<0xB1>(pk);
      __builtin_amdgcn_raw_buffer_store_b128(u32x4{(unsigned)__builtin_amdgcn_bitop3_b32(tag, pk, pk2, 0x96), __float_as_uint(lse), pk, pk2}, wr, poff, 0, SC1);
    }
    if (wave == 0) {
      const __amdgpu_buffer_rsrc_t orr = srd(out, HQ * D * 2);
      unsigned long long p0, p1, p2, p3;
      unsigned cnt;
#define QP " quad_perm:[1,0,3,2] row_mask:0xf bank_mask:0xf bound_ctrl:1\n\t"
#define RR4 " row_ror:4 row_mask:0xf bank_mask:0xf bound_ctrl:1\n\t"
#define RR8 " row_ror:8 row_mask:0xf bank_mask:0xf bound_ctrl:1\n\t"
      asm volatile(
          "s_mov_b64 %0, -1\n\ts_mov_b64 %1, 0\n\ts_mov_b64 %2, 0\n\ts_mov_b64 %3, 0\n\t"
          "v_mbcnt_lo_u32_b32 v205, -1, 0\n\tv_mbcnt_hi_u32_b32 v205, -1, v205\n\tv_mov_b32 v209, v205\n\tv_mov_b32 v213, v205\n\t"
          "s_sleep 8\n\t"
          "s_mov_b32 %4, 0x200000\n"
          ".Lpoll%=:\n\t"
          "s_mov_b64 exec, %0\n\tbuffer_load_dwordx4 v[200:203], %6, %7, 0 offen sc1\n\t"
          "s_mov_b64 exec, %1\n\tbuffer_load_dwordx4 v[204:207], %6, %7, %10 offen sc1\n\t"
          "s_mov_b64 exec, %2\n\tbuffer_load_dwordx4 v[208:211], %6, %7, %11 offen sc1\n\t"
          "s_mov_b64 exec, %3\n\tbuffer_load_dwordx4 v[212:215], %6, %7, %12 offen sc1\n\t"
          "s_mov_b64 exec, -1\n\t"
          "s_waitcnt vmcnt(0)\n\t"
          "v_max3_f32 v221, v201, v205, %13\n\t"
          "v_max3_f32 v221, v221, v209, v213\n\t"
          "v_xor_b32_dpp v216, v201, v201" QP
          "v_xor_b32_dpp v217, v205, v205" QP
          "v_max_f32_dpp v221, v221, v221" RR4
          "v_xor_b32_dpp v218, v209, v209" QP
          "v_xor_b32_dpp v219, v213, v213" QP
          "v_max_f32_dpp v221, v221, v221" RR8
          "v_bitop3_b32 v224, v200, v202, v203 bitop3:0x96\n\t"
          "v_mov_b32 v222, v221\n\t"
          "v_bitop3_b32 v225, v204, v206, v207 bitop3:0x96\n\t"
          "v_bitop3_b32 v226, v208, v210, v211 bitop3:0x96\n\t"
          "v_permlane16_swap_b32 v221, v222\n\t"
          "v_bitop3_b32 v227, v212, v214, v215 bitop3:0x96\n\t"
          "v_max_f32 v221, v221, v222\n\t"
          "v_bitop3_b32 v216, v224, v216, %5 bitop3:0xde\n\t"
          "v_mov_b32 v222, v221\n\t"
          "v_bitop3_b32 v217, v225, v217, %5 bitop3:0xde\n\t"
          "v_bitop3_b32 v218, v226, v218, %5 bitop3:0xde\n\t"
          "v_permlane32_swap_b32 v221, v222\n\t"
          "v_bitop3_b32 v219, v227, v219, %5 bitop3:0xde\n\t"
          "v_or3_b32 v220, v216, v217, v218\n\t"
          "v_or_b32 v220, v220, v219\n\t"
          "v_cmp_ne_u32 vcc, 0, v220\n\t"
          "s_cbranch_vccz .Ldone%=\n\t"
          "v_or_b32_dpp v216, v216, v216" QP
          "v_or_b32_dpp v217, v217, v217" QP
          "v_or_b32_dpp v218, v218, v218" QP
          "v_or_b32_dpp v219, v219, v219" QP
          "v_cmp_ne_u32 %0, 0, v216\n\t"
          "v_cmp_ne_u32 %1, 0, v217\n\t"
          "v_cmp_ne_u32 %2, 0, v218\n\t"
          "v_cmp_ne_u32 %3, 0, v219\n\t"
          "s_bcnt1_i32_b64 vcc_lo, %0\n\t"
          "s_cmp_gt_u32 vcc_lo, 32\n\t"
          "s_cselect_b64 %1, 0, %1\n\ts_cselect_b64 %2, 0, %2\n\ts_cselect_b64 %3, 0, %3\n\t"
          "s_cbranch_scc0 .Lns%=\n\t"
          "s_sleep 6\n"
          ".Lns%=:\n\t"
          "s_sub_u32 %4, %4, 1\n\t"
          "s_cbranch_scc0 .Lpoll%=\n"
          ".Ldone%=:\n\t"
          "v_max_f32 v221, v221, v222\n\t"
          "v_lshlrev_b32 v224, 16, v202\n\t"
          "v_sub_f32 v201, v201, v221\n\t"
          "v_sub_f32 v205, v205, v221\n\t"
          "v_sub_f32 v209, v209, v221\n\t"
          "v_sub_f32 v213, v213, v221\n\t"
          "v_exp_f32 v200, v201\n\t"
          "v_exp_f32 v204, v205\n\t"
          "v_exp_f32 v208, v209\n\t"
          "v_exp_f32 v212, v213\n\t"
          "v_and_b32 v225, %14, v202\n\t"
          "v_lshlrev_b32 v226, 16, v203\n\t"
          "v_and_b32 v227, %14, v203\n\t"
          "v_lshlrev_b32 v228, 16, v206\n\t"
          "v_and_b32 v229, %14, v206\n\t"
          "v_pk_mul_f32 v[224:225], v[224:225], v[200:201] op_sel_hi:[1,0]\n\t"
          "v_pk_mul_f32 v[226:227], v[226:227], v[200:201] op_sel_hi:[1,0]\n\t"
          "v_lshlrev_b32 v230, 16, v207\n\t"
          "v_and_b32 v231, %14, v207\n\t"
          "v_add_f32 v223, v200, v204\n\t"
          "v_pk_fma_f32 v[224:225], v[228:229], v[204:205], v[224:225] op_sel_hi:[1,0,1]\n\t"
          "v_pk_fma_f32 v[226:227], v[230:231], v[204:205], v[226:227] op_sel_hi:[1,0,1]\n\t"
          "v_lshlrev_b32 v228, 16, v210\n\t"
          "v_and_b32 v229, %14, v210\n\t"
          "v_lshlrev_b32 v230, 16, v211\n\t"
          "v_and_b32 v231, %14, v211\n\t"
          "v_add_f32 v222, v208, v212\n\t"
          "v_pk_fma_f32 v[224:225], v[228:229], v[208:209], v[224:225] op_sel_hi:[1,0,1]\n\t"
          "v_pk_fma_f32 v[226:227], v[230:231], v[208:209], v[226:227] op_sel_hi:[1,0,1]\n\t"
          "v_add_f32 v223, v223, v222\n\t"
          "v_lshlrev_b32 v228, 16, v214\n\t"
          "v_and_b32 v229, %14, v214\n\t"
          "v_add_f32_dpp v223, v223, v223" RR4
          "v_lshlrev_b32 v230, 16, v215\n\t"
          "v_and_b32 v231, %14, v215\n\t"
          "v_add_f32_dpp v223, v223, v223" RR8
          "v_pk_fma_f32 v[224:225], v[228:229], v[212:213], v[224:225] op_sel_hi:[1,0,1]\n\t"
          "v_pk_fma_f32 v[226:227], v[230:231], v[212:213], v[226:227] op_sel_hi:[1,0,1]\n\t"
          "v_mov_b32 v222, v223\n\t"
          "s_nop 0\n\t"
          "v_permlane32_swap_b32 v224, v225\n\t"
          "v_permlane32_swap_b32 v226, v227\n\t"
          "v_permlane16_swap_b32 v223, v222\n\t"
          "v_add_f32 v224, v224, v225\n\t"
          "v_add_f32 v226, v226, v227\n\t"
          "v_add_f32 v223, v223, v222\n\t"
          "s_nop 0\n\t"
          "v_mov_b32 v222, v223\n\t"
          "v_permlane16_swap_b32 v224, v226\n\t"
          "s_nop 0\n\t"
          "v_permlane32_swap_b32 v223, v222\n\t"
          "v_add_f32 v224, v224, v226\n\t"
          "v_add_f32 v223, v223, v222\n\t"
          "v_rcp_f32 v222, v223\n\t"
          "v_add_f32_dpp v224, v224, v224" RR4
          "v_cmp_lt_f32 vcc, 0, v223\n\t"
          "s_nop 0\n\t"
          "v_add_f32_dpp v224, v224, v224" RR8
          "v_cndmask_b32 v222, 0, v222, vcc\n\t"
          "v_mul_f32 v224, v224, v222\n\t"
          "v_cvt_pk_bf16_f32 v224, v224, v224\n\t"
          "buffer_store_short v224, %8, %9, 0 offen sc1"
          : "=&s"(p0), "=&s"(p1), "=&s"(p2), "=&s"(p3), "=&s"(cnt)
          : "v"(tag), "v"(gbase), "s"(wr), "v"(ooff), "s"(orr), "s"(3 * 4096), "s"(1 * 4096), "s"(2 * 4096), "s"(-3.0e38f), "s"(0xffff0000u)
          : "memory", "vcc", "scc", "v200", "v201", "v202", "v203", "v204", "v205", "v206", "v207", "v208", "v209", "v210", "v211", "v212", "v213", "v214", "v215",
            "v216", "v217", "v218", "v219", "v220", "v221", "v222", "v223", "v224", "v225", "v226", "v227", "v228", "v229", "v230", "v231");
      if (idx == 0 && tid == 0) {
        __hip_atomic_store((gu32*)(ws + WS_EA + hp * 128), ea + 1u, __ATOMIC_RELAXED, __HIP_MEMORY_SCOPE_AGENT);
        __hip_atomic_store((gu32*)(ws + WS_EB + hp * 128), eb + 1u, __ATOMIC_RELAXED, __HIP_MEMORY_SCOPE_AGENT);
      }
    }
  };

  if (__builtin_expect(nfull <= WIN - 1, 1)) {
    window(std::true_type{}, std::integral_constant<int, 0>{});
    finish();
  } else {
    window(std::false_type{}, std::integral_constant<int, 0>{});
    __builtin_amdgcn_s_setprio(3);
    window(std::true_type{}, std::integral_constant<int, WIN - 1>{});
    finish();
  }
}

extern "C" void kernel_launch(void* const* d_in, const int* in_sizes, int n_in, void* d_out, int out_size, void* d_ws, size_t ws_size, hipStream_t stream) {
  if (n_in != 5 || in_sizes[0] != HQ * D || in_sizes[1] < NBPS * (PAGEB / 2) || in_sizes[2] != in_sizes[1] || in_sizes[3] != NBPS || in_sizes[4] != 1 ||
      out_size != HQ * D || ws_size < WS_END) return;
  attn_decode<<<dim3(NWG), dim3(NWAVE * 64), 0, stream>>>(d_in[0], d_in[1], d_in[2], d_in[3], d_in[4], d_ws, d_out, d_ws);
}
```
